```python
import torch

def _softmax(x, axis=-1):
    return torch.softmax(x, dim=axis)

def _gelu(x):
    return torch.nn.functional.gelu(x, approximate='tanh')

def _silu(x):
    return torch.nn.functional.silu(x)

def _sigmoid(x):
    return torch.sigmoid(x)

def _layernorm(x, axis=-1, eps=1e-05):
    m = x.mean(dim=axis, keepdim=True)
    v = x.var(dim=axis, keepdim=True, unbiased=False)
    return (x - m) / torch.sqrt(v + eps)

def _scatter_add(tgt, idx, src, axis=0):
    return tgt.clone().index_add_(axis, idx, src)

def _segment_sum(src, seg, n):
    out = torch.zeros((n,) + src.shape[1:], dtype=src.dtype)
    return out.index_add_(0, seg, src)

def _pad(x, pw):
    flat = tuple((v for pair in reversed(pw) for v in pair))
    return torch.nn.functional.pad(x, flat)
kernel_name = 'adaLN_cond__gqa_block_cached__geglu_ffn__decode'

def setup_inputs():

    _w = lambda a: (lambda t: t.to(torch.bfloat16) if t.is_floating_point() else t)(torch.as_tensor(a))

    def _A_setup():
        B, Sq, D = (4, 1, 512)
        Hq, Hkv, Sk, Dh = (32, 8, 8192, 128)
        assert Hq % Hkv == 0, (Hq, Hkv)
        return {'x': _w((lambda *s: torch.randn(*s, dtype=torch.bfloat16) * 0.1)(B, Sq, D)), 'k_cache': _w((lambda *s: torch.randn(*s, dtype=torch.bfloat16) * 0.8)(B, Hkv, Sk, Dh)), 'v_cache': _w((lambda *s: torch.randn(*s, dtype=torch.bfloat16) * 1.6)(B, Hkv, Sk, Dh)), 'wq': _w((lambda *s: torch.randn(*s, dtype=torch.bfloat16) * 0.8)(D, Hq * Dh) * D ** (-0.5)), 'wo': _w((lambda *s: torch.randn(*s, dtype=torch.bfloat16) * 1.6)(Hq * Dh, D) * (Hq * Dh) ** (-0.5))}
    _A_in = _A_setup()

    def _B_setup():
        B, S, D, F = (4, 1, 512, 1792)
        return {'x': _w((lambda *s: torch.randn(*s, dtype=torch.bfloat16) * 0.1)(B, S, D)), 'w_gate': _w((lambda *s: torch.randn(*s, dtype=torch.bfloat16) * 0.1)(D, F)), 'w_up': _w((lambda *s: torch.randn(*s, dtype=torch.bfloat16) * 0.1)(D, F)), 'w_down': _w((lambda *s: torch.randn(*s, dtype=torch.bfloat16) * 0.1)(F, D))}
    _B_in = _B_setup()
    return {'A_x': _A_in['x'], 'A_k_cache': _A_in['k_cache'], 'A_v_cache': _A_in['v_cache'], 'A_wq': _A_in['wq'], 'A_wo': _A_in['wo'], 'B_x': _B_in['x'], 'B_w_gate': _B_in['w_gate'], 'B_w_up': _B_in['w_up'], 'B_w_down': _B_in['w_down'], 'c': _w((lambda *s: torch.randn(*s, dtype=torch.bfloat16) * 0.8)(4, 512) * 8.0), 'w_mod': _w((lambda *s: torch.randn(*s, dtype=torch.bfloat16) * 0.8)(512, 6 * 512) * 512 ** (-0.5) * 8.0)}

def reference(A_x, A_k_cache, A_v_cache, A_wq, A_wo, B_x, B_w_gate, B_w_up, B_w_down, c, w_mod):

    def _A_ref(x, k_cache, v_cache, wq, wo):
        B, Sq, D = x.shape
        Hq, Dh = (32, 128)
        Hkv = k_cache.shape[1]
        rep = Hq // Hkv
        q = (lambda x, *d: x.permute(*d))((x @ wq).reshape(B, Sq, Hq, Dh), 0, 2, 1, 3)
        k = (lambda x, n, axis: torch.repeat_interleave(x, n, dim=axis))(k_cache, rep, axis=1)
        v = (lambda x, n, axis: torch.repeat_interleave(x, n, dim=axis))(v_cache, rep, axis=1)
        scores = torch.einsum('bhqd,bhkd->bhqk', q, k) * Dh ** (-0.5)
        p = _softmax(scores, axis=-1)
        o = torch.einsum('bhqk,bhkd->bhqd', p, v)
        return (lambda x, *d: x.permute(*d))(o, 0, 2, 1, 3).reshape(B, Sq, Hq * Dh) @ wo

    def _B_ref(x, w_gate, w_up, w_down):
        return _gelu(x @ w_gate) * (x @ w_up) @ w_down
    x = A_x
    mod = (_silu(c) @ w_mod).reshape(c.shape[0], 6, 1, -1)
    sa, ca, ga, sb, cb, gb = (mod[:, 0].clone(), mod[:, 1].clone(), mod[:, 2].clone(), mod[:, 3].clone(), mod[:, 4].clone(), mod[:, 5].clone())
    h = _layernorm(x, axis=-1) * (1 + ca) + sa
    x = x + ga * _A_ref(h, A_k_cache, A_v_cache, A_wq, A_wo)
    h = _layernorm(x, axis=-1) * (1 + cb) + sb
    x = x + gb * _B_ref(h, B_w_gate, B_w_up, B_w_down)
    return x
```

```cpp
#include <hip/hip_runtime.h>
#include <hip/hip_bf16.h>
#include <stdint.h>
#define MIMIC 0
typedef unsigned short u16;
__device__ __forceinline__ float bf2f(u16 v){ return __uint_as_float(((unsigned)v)<<16); }
__device__ __forceinline__ u16 f2bf(float f){ unsigned u=__float_as_uint(f); u += 0x7FFFu + ((u>>16)&1u); return (u16)(u>>16); }
__device__ __forceinline__ float rb(float f){
  return f;
}
constexpr int B=4,D=512,HQ=32,HKV=8,SK=8192,DH=128,F=1792;
constexpr int NS=8;
__global__ void k_mod(const u16* c,const u16* wmod,float* mod){
  int n=blockIdx.x*256+threadIdx.x;
  __shared__ float sc[B][D];
  for(int i=threadIdx.x;i<B*D;i+=256){ float v=bf2f(c[i]); float s=v/(1.f+expf(-v)); sc[i/D][i%D]=rb(s);}
  __syncthreads();
  float a[B]={0,0,0,0};
  for(int k=0;k<D;k++){ float w=bf2f(wmod[(size_t)k*3072+n]); for(int b=0;b<B;b++) a[b]+=sc[b][k]*w; }
  for(int b=0;b<B;b++) mod[b*3072+n]=rb(a[b]);
}
__global__ void k_ln(const float* xin_f,const u16* xin_b,const float* mod,int sidx,int cidx,float* h){
  int b=blockIdx.x; int t=threadIdx.x;
  __shared__ float red[512];
  float x = xin_f? xin_f[b*D+t] : bf2f(xin_b[b*D+t]);
  red[t]=x; __syncthreads();
  for(int s=256;s>0;s>>=1){ if(t<s) red[t]+=red[t+s]; __syncthreads(); }
  float mean=red[0]/D; __syncthreads();
  float d=x-mean; red[t]=d*d; __syncthreads();
  for(int s=256;s>0;s>>=1){ if(t<s) red[t]+=red[t+s]; __syncthreads(); }
  float var=red[0]/D;
  float m=rb(mean), v=rb(var);
  float num=rb(x-m); float den=rb(sqrtf(rb(v+1e-5f)));
  float ln=rb(num/den);
  float cc=mod[b*3072+cidx*512+t], ss=mod[b*3072+sidx*512+t];
  h[b*D+t]=rb(rb(ln*rb(1.f+cc))+ss);
}
__global__ void k_gemv(const float* in,const u16* w,float* out,int K,int N){
  int n=blockIdx.x*64+threadIdx.x%64; int ks=threadIdx.x/64;
  __shared__ float red[4][B][64];
  float a[B]={0,0,0,0};
  for(int k=ks;k<K;k+=4){ float ww=bf2f(w[(size_t)k*N+n]); for(int b=0;b<B;b++) a[b]+=in[b*K+k]*ww; }
  for(int b=0;b<B;b++) red[ks][b][threadIdx.x%64]=a[b];
  __syncthreads();
  if(ks==0) for(int b=0;b<B;b++) out[b*N+n]=rb(red[0][b][threadIdx.x]+red[1][b][threadIdx.x]+red[2][b][threadIdx.x]+red[3][b][threadIdx.x]);
}
__global__ void k_attn(const float* q,const u16* kc,const u16* vc,float* o){
  int bh=blockIdx.x; int b=bh/HQ,h=bh%HQ,kv=h/(HQ/HKV);
  __shared__ float s[SK]; __shared__ float qs[DH]; __shared__ float red[256];
  int t=threadIdx.x;
  if(t<DH) qs[t]=q[b*4096+h*DH+t];
  __syncthreads();
  const u16* kb=kc+((size_t)(b*HKV+kv))*SK*DH; const u16* vb=vc+((size_t)(b*HKV+kv))*SK*DH;
  float mx=-INFINITY;
  for(int j=t;j<SK;j+=256){ float a=0; for(int d=0;d<DH;d++) a+=qs[d]*bf2f(kb[(size_t)j*DH+d]); a=rb(rb(a)*0.08838834764831845f); s[j]=a; mx=fmaxf(mx,a);}
  red[t]=mx; __syncthreads();
  for(int st=128;st>0;st>>=1){ if(t<st) red[t]=fmaxf(red[t],red[t+st]); __syncthreads(); }
  mx=red[0]; __syncthreads();
  float sum=0; for(int j=t;j<SK;j+=256){ float e=expf(s[j]-mx); s[j]=e; sum+=e; }
  red[t]=sum; __syncthreads();
  for(int st=128;st>0;st>>=1){ if(t<st) red[t]+=red[t+st]; __syncthreads(); }
  sum=red[0]; __syncthreads();
  for(int j=t;j<SK;j+=256) s[j]=rb(s[j]/sum);
  __syncthreads();
  int d=t%DH, half=t/DH; float a=0;
  for(int j=half;j<SK;j+=2) a+=s[j]*bf2f(vb[(size_t)j*DH+d]);
  red[t]=a; __syncthreads();
  if(t<DH) o[b*4096+h*DH+t]=rb(red[t]+red[t+DH]);
}
__global__ void k_res(const u16* x,const float* xf,const float* y,const float* mod,int gidx,float* x2,u16* out){
  int i=blockIdx.x*256+threadIdx.x; int b=i/D,t=i%D;
  float xv= xf? xf[i]:bf2f(x[i]);
  float r=rb(xv+rb(mod[b*3072+gidx*512+t]*y[i]));
  if(x2) x2[i]=r; if(out) out[i]=f2bf(r);
}
__global__ void k_glu(const float* g,const float* u,float* hid){
  int i=blockIdx.x*256+threadIdx.x;
  float x=g[i]; float ge=0.5f*x*(1.f+tanhf(0.7978845608028654f*(x+0.044715f*x*x*x)));
  hid[i]=rb(rb(ge)*u[i]);
}
extern "C" void kernel_launch(void* const* d_in,const int* in_sizes,int n_in,void* d_out,int out_size,void* d_ws,size_t ws_size,hipStream_t stream){
  const u16* Ax=(const u16*)d_in[0]; const u16* kc=(const u16*)d_in[1]; const u16* vc=(const u16*)d_in[2];
  const u16* wq=(const u16*)d_in[3]; const u16* wo=(const u16*)d_in[4];
  const u16* wg=(const u16*)d_in[6]; const u16* wu=(const u16*)d_in[7]; const u16* wd=(const u16*)d_in[8];
  const u16* c=(const u16*)d_in[9]; const u16* wmod=(const u16*)d_in[10];
  float* ws=(float*)d_ws;
  float* mod=ws; float* h=mod+4*3072; float* q=h+2048; float* o=q+16384; float* att=o+16384; float* x2=att+2048; float* h2=x2+2048;
  float* g=h2+2048; float* u=g+4*F; float* hid=u+4*F; float* dn=hid+4*F;
  k_mod<<<12,256,0,stream>>>(c,wmod,mod);
  k_ln<<<4,512,0,stream>>>(nullptr,Ax,mod,0,1,h);
  k_gemv<<<4096/64,256,0,stream>>>(h,wq,q,512,4096);
  k_attn<<<128,256,0,stream>>>(q,kc,vc,o);
  k_gemv<<<512/64,256,0,stream>>>(o,wo,att,4096,512);
  k_res<<<8,256,0,stream>>>(Ax,nullptr,att,mod,2,x2,nullptr);
  k_ln<<<4,512,0,stream>>>(x2,nullptr,mod,3,4,h2);
  k_gemv<<<F/64,256,0,stream>>>(h2,wg,g,512,F);
  k_gemv<<<F/64,256,0,stream>>>(h2,wu,u,512,F);
  k_glu<<<4*F/256,256,0,stream>>>(g,u,hid);
  k_gemv<<<512/64,256,0,stream>>>(hid,wd,dn,F,512);
  k_res<<<8,256,0,stream>>>(nullptr,x2,dn,mod,5,nullptr,(u16*)d_out);
}
```

```cpp
#include <hip/hip_runtime.h>
#include <hip/hip_bf16.h>
#include <stdint.h>

#define GAS __attribute__((address_space(1)))
#define LAS __attribute__((address_space(3)))
typedef unsigned v4u __attribute__((ext_vector_type(4)));
typedef unsigned v2u __attribute__((ext_vector_type(2)));
typedef float f32x4 __attribute__((ext_vector_type(4)));
typedef short s16x4 __attribute__((ext_vector_type(4)));
typedef __bf16 bf16x8 __attribute__((ext_vector_type(8)));
typedef __bf16 bf16x2 __attribute__((ext_vector_type(2)));
typedef LAS unsigned char* lptr;

#define PACE 4
#define STAMPS 0

namespace {
constexpr int NCU = 256, NTHR = 512;
constexpr unsigned KV_BYTES = 4u * 8u * 8192u * 128u * 2u;
constexpr unsigned WS_EPOCH = 0, WS_READY = 4096, WS_H = 8192, WS_Q = 16384, WS_GATE = WS_Q + 65536, WS_STAT = WS_GATE + 16384, WS_ATT = WS_STAT + 8192,
                   WS_PART = WS_ATT + 65536, WS_DOWN = WS_PART + 256 * 2048, WS_END = WS_DOWN + 56 * 8192, WS_STAMP = WS_END + 65536;
constexpr int RS = 1088;
constexpr int L_W = 0, L_WD = 65536, L_MRG = 98304, L_GATE = L_MRG + 17408, L_ACT = L_GATE + 16384, L_X = L_ACT + 4352, L_Q = L_X + 4096, L_RED = L_Q + 1024,
              L_MISC = L_RED + 8192, L_BYTES = L_MISC + 1024;
constexpr int MRG_H = 528, MRG_W = 4 * MRG_H;
constexpr int M_LN = L_MISC, M_ML = L_MISC + 64, M_HID = L_MISC + 320, M_FLAG = L_MISC + 576;
constexpr unsigned SENT = 0xFFFFFFFFu;
constexpr unsigned SPIN_MAX = 1u << 15;
constexpr int AUX_NT = 2, AUX_SC1 = 16;
constexpr float LOG2E = 1.4426950408889634f;
constexpr int J_H = 0, J_G = 2, J_Q = 6, J_B = 14, J_C = 22, J_F_END = 26, J_C_END = 29;

struct Params { const void* x; const void* k; const void* v; const void* wq; const void* wo; const void* wg; const void* wu; const void* wd; const void* c; const void* wm; void* out; void* ws; };

__device__ __forceinline__ float bf_lo(unsigned u) { return __builtin_bit_cast(float, u << 16); }
__device__ __forceinline__ float bf_hi(unsigned u) { return __builtin_bit_cast(float, u & 0xffff0000u); }
__device__ __forceinline__ unsigned pk2(float a, float b) { bf16x2 r = {(__bf16)a, (__bf16)b}; return __builtin_bit_cast(unsigned, r); }
__device__ __forceinline__ float rb(float a) { return bf_lo(pk2(a, a)); }
template <int CTRL> __device__ __forceinline__ float dpp(float x) { return __builtin_bit_cast(float, __builtin_amdgcn_mov_dpp(__builtin_bit_cast(int, x), CTRL, 0xf, 0xf, true)); }
template <int CTRL> __device__ __forceinline__ unsigned dppu(unsigned x) { return (unsigned)__builtin_amdgcn_mov_dpp((int)x, CTRL, 0xf, 0xf, true); }
constexpr int XOR1 = 0xB1, XOR2 = 0x4E, HMIR = 0x141, ROR4 = 0x124, ROR8 = 0x128;
__device__ __forceinline__ float x16_sum(float x) { auto s = __builtin_amdgcn_permlane16_swap(__float_as_uint(x), __float_as_uint(x), false, false); return __uint_as_float(s[0]) + __uint_as_float(s[1]); }
__device__ __forceinline__ float x32_sum(float x) { auto s = __builtin_amdgcn_permlane32_swap(__float_as_uint(x), __float_as_uint(x), false, false); return __uint_as_float(s[0]) + __uint_as_float(s[1]); }
__device__ __forceinline__ float x16_max(float x) { auto s = __builtin_amdgcn_permlane16_swap(__float_as_uint(x), __float_as_uint(x), false, false); return fmaxf(__uint_as_float(s[0]), __uint_as_float(s[1])); }
__device__ __forceinline__ float x32_max(float x) { auto s = __builtin_amdgcn_permlane32_swap(__float_as_uint(x), __float_as_uint(x), false, false); return fmaxf(__uint_as_float(s[0]), __uint_as_float(s[1])); }
__device__ __forceinline__ float wave_sum(float x) { x += dpp<XOR1>(x); x += dpp<XOR2>(x); x += dpp<HMIR>(x); x += dpp<ROR8>(x); return x32_sum(x16_sum(x)); }
__device__ __forceinline__ f32x4 mfma(v4u a, v4u b, f32x4 c) { return __builtin_amdgcn_mfma_f32_16x16x32_bf16(__builtin_bit_cast(bf16x8, a), __builtin_bit_cast(bf16x8, b), c, 0, 0, 0); }
__device__ __forceinline__ v2u tr_read(lptr p) { s16x4 r = __builtin_amdgcn_ds_read_tr16_b64_v4i16((LAS s16x4*)p); return __builtin_bit_cast(v2u, r); }
__device__ __forceinline__ v4u tr_frag(lptr p0, lptr p1) { v2u a = tr_read(p0), b = tr_read(p1); return v4u{a.x, a.y, b.x, b.y}; }
__device__ __forceinline__ v4u lds128(lptr p) { return *(LAS v4u*)p; }
__device__ __forceinline__ void bar() { asm volatile("s_waitcnt lgkmcnt(0)\n\ts_barrier" ::: "memory"); }
__device__ __forceinline__ void glds16(const void* gsrc, unsigned lds_dst) {
  unsigned keep;
  asm volatile("s_mov_b32 %0, m0\n\ts_mov_b32 m0, %2\n\ts_nop 0\n\tglobal_load_lds_dwordx4 %1, off nt\n\ts_mov_b32 m0, %0" : "=&s"(keep) : "v"(gsrc), "s"(lds_dst) : "memory");
}
__device__ __forceinline__ unsigned nosent(float f) { unsigned u = __float_as_uint(f); return u == SENT ? SENT - 1u : u; }
__device__ __forceinline__ unsigned nosentu(unsigned u) { return u == SENT ? SENT - 1u : u; }
__device__ __forceinline__ unsigned umax4(v4u a) { return max(max(a.x, a.y), max(a.z, a.w)); }
__device__ __forceinline__ bool all_lanes(bool ok) { return __builtin_amdgcn_ballot_w64(!ok) == 0ull; }
__device__ __forceinline__ float sel4(f32x4 d, int g) { return g == 0 ? d[0] : g == 1 ? d[1] : g == 2 ? d[2] : d[3]; }
#define RSRC(p, n) __builtin_amdgcn_make_buffer_rsrc((void*)(p), 0, (int)(n), 0x00020000)
#define LD_SC1(off) __builtin_amdgcn_raw_buffer_load_b128(rws, (off), 0, AUX_SC1)
#define ST_SC1(v, off) __builtin_amdgcn_raw_buffer_store_b128((v), rws, (off), 0, AUX_SC1)
#define FENCE() __builtin_amdgcn_sched_barrier(0)
#define STAMP(i) do { } while (0)

__device__ __forceinline__ int img_chunk(int lane, int row) { return ((((lane & 7) >> 1) ^ ((row >> 1) & 3)) << 1) | (lane & 1); }
__device__ __forceinline__ int frag_base(int lane, int ct) { const int g = lane >> 4, q = (lane >> 2) & 3; return (4 * g + q) * 128 + ((ct ^ ((2 * g + (q >> 1)) & 3)) * 32) + (lane & 3) * 8; }
__device__ __forceinline__ v4u w_frag(lptr base, int s) { return tr_frag(base + s * 4096, base + s * 4096 + 2048); }
__device__ __forceinline__ int act_off(int k) { return (k >> 5) * 64 + ((k >> 2) & 3) * 16 + ((k >> 4) & 1) * 8; }
__device__ __forceinline__ f32x4 gemv_tile(lptr W, lptr act, int lane, int ct) {
  lptr ap = act + (lane & 3) * RS + (lane >> 4) * 16;
  lptr wp = W + frag_base(lane, ct);
  f32x4 a0 = {0.f, 0.f, 0.f, 0.f}, a1 = {0.f, 0.f, 0.f, 0.f};
#pragma unroll
  for (int s = 0; s < 16; s += 2) {
    a0 = mfma(lds128(ap + s * 64), w_frag(wp, s), a0);
    a1 = mfma(lds128(ap + s * 64 + 64), w_frag(wp, s + 1), a1);
  }
  return a0 + a1;
}
__device__ __forceinline__ void ln_wave_stats(const float (&v)[4], float& sum, float& m2) {
  sum = wave_sum((v[0] + v[1]) + (v[2] + v[3]));
  const float mw = sum * (1.f / 256.f);
  const float d0 = v[0] - mw, d1 = v[1] - mw, d2 = v[2] - mw, d3 = v[3] - mw;
  m2 = wave_sum((d0 * d0 + d1 * d1) + (d2 * d2 + d3 * d3));
}
__device__ __forceinline__ void ln_row(lptr L, int b, float& mean_r, float& den_r) {
  const f32x4 s = *(LAS f32x4*)(L + M_LN + b * 16);
  const float dm = (s.x - s.z) * (1.f / 256.f);
  const float mean = (s.x + s.z) * (1.f / 512.f);
  const float var = (s.y + s.w + dm * dm * 128.f) * (1.f / 512.f);
  mean_r = rb(mean);
  den_r = rb(__builtin_sqrtf(rb(rb(var) + 1e-5f)));
}
__device__ __forceinline__ float ln_mod(float x, float mean_r, float den_r, float cc, float ss) {
  const float ln = rb(rb(x - mean_r) / den_r);
  return rb(rb(ln * rb(1.f + cc)) + ss);
}

__global__ void __launch_bounds__(NTHR, 2) adaln_block_kernel(Params p) {
  __shared__ __attribute__((aligned(16))) unsigned char smem[L_BYTES + (STAMPS ? 1024 : 0)];
  lptr L = (lptr)smem;
  const unsigned lds0 = (unsigned)(uintptr_t)L;
  const int tid = threadIdx.x, lane = tid & 63, w = __builtin_amdgcn_readfirstlane(tid >> 6);
  const int cu = blockIdx.x, X = cu & 7, J = cu >> 3;
  const int g = lane >> 4, n = lane & 15;
  const auto rws = RSRC(p.ws, WS_END);
  const auto rk = RSRC(p.k, KV_BYTES);
  const auto rv = RSRC(p.v, KV_BYTES);
  const bool roleH = J < J_G, roleG = J >= J_G && J < J_Q, roleM = J < J_Q, roleQ = J >= J_Q && J < J_B, roleB = J >= J_B && J < J_C, roleC = J >= J_C && J < J_C_END;
  STAMP(0);
  const unsigned long long ep = __hip_atomic_load((GAS unsigned long long*)p.ws, __ATOMIC_RELAXED, __HIP_MEMORY_SCOPE_AGENT);
  v2u cv = {0u, 0u}, xv = {0u, 0u};
  if (roleM) {
    cv = *(const GAS v2u*)((const GAS unsigned char*)p.c + ((tid >> 7) * 512 + 4 * (tid & 127)) * 2);
    if (roleH) xv = *(const GAS v2u*)((const GAS unsigned char*)p.x + ((tid >> 7) * 512 + 4 * (tid & 127)) * 2);
  }
  if (roleM || roleQ) {
    const int r0 = 64 * w + (lane >> 3), ch = img_chunk(lane, r0);
    const GAS unsigned char* s;
    size_t rstride;
    if (roleH) { s = (const GAS unsigned char*)p.wm + ((size_t)r0 * 3072 + (ch >> 2) * 512 + 32 * (2 * X + J) + 8 * (ch & 3)) * 2; rstride = 3072 * 2; }
    else if (roleG) { s = (const GAS unsigned char*)p.wm + ((size_t)r0 * 3072 + 1024 + 64 * (4 * X + (J - J_G)) + 8 * ch) * 2; rstride = 3072 * 2; }
    else { s = (const GAS unsigned char*)p.wq + ((size_t)r0 * 4096 + 64 * (8 * X + (J - J_Q)) + 8 * ch) * 2; rstride = 4096 * 2; }
#pragma unroll
    for (int e = 0; e < 8; ++e) glds16((const void*)(s + (size_t)e * 8 * rstride), lds0 + L_W + w * 8192 + e * 1024);
    asm volatile("s_barrier" ::: "memory");
  }
  if (w == 1) {
    const v4u sent = {SENT, SENT, SENT, SENT};
    ST_SC1(sent, WS_PART + cu * 2048 + lane * 16);
    ST_SC1(sent, WS_PART + cu * 2048 + 1024 + lane * 16);
    if (lane < 2) ST_SC1(sent, WS_STAT + cu * 32 + lane * 16);
    if (roleB) ST_SC1(sent, WS_ATT + ((X * 4 + g) * 512 + 64 * (J - J_B) + 4 * n) * 4);
    if (roleG && lane < 32) ST_SC1(sent, WS_GATE + (4 * X + (J - J_G)) * 512 + lane * 16);
    if (roleC) {
#pragma unroll
      for (int e = 0; e < 8; ++e) ST_SC1(sent, WS_DOWN + (7 * X + (J - J_C)) * 8192 + e * 1024 + lane * 16);
    }
  }
  FENCE();
  const unsigned kvbase = (((unsigned)(J >> 3) * 8u + (unsigned)X) * 8192u + (unsigned)(J & 7) * 1024u) * 256u + (unsigned)w * 4096u;
  const unsigned koff = (4 * (lane & 3) + ((lane >> 2) & 1)) * 256 + (lane >> 3) * 16;
  v4u kf[8][4];
#define K_ISSUE(t) do { unsigned ko_ = koff; asm volatile("" : "+v"(ko_)); _Pragma("unroll") for (int q_ = 0; q_ < 4; ++q_) kf[t][q_] = __builtin_amdgcn_raw_buffer_load_b128(rk, ko_ + (q_ >> 1) * 512 + (q_ & 1) * 128, kvbase + (t) * 32768, AUX_NT); FENCE(); } while (0)
  K_ISSUE(0); K_ISSUE(1);
  if (tid == 0) *(LAS unsigned*)(L + M_FLAG) = 0u;
  const unsigned long long E = ep + 1ull;
  const unsigned ta = __builtin_amdgcn_readfirstlane((unsigned)E), tb2 = __builtin_amdgcn_readfirstlane(((unsigned)E * 0x9E3779B1u) ^ (unsigned)(E >> 32));
  if (roleM) {
    const int tb = tid >> 7, tk = 4 * (tid & 127);
    float cs[4] = {bf_lo(cv.x), bf_hi(cv.x), bf_lo(cv.y), bf_hi(cv.y)};
#pragma unroll
    for (int e = 0; e < 4; ++e) cs[e] = cs[e] / (1.f + __expf(-cs[e]));
    *(LAS v2u*)(L + L_ACT + tb * RS + act_off(tk)) = v2u{pk2(cs[0], cs[1]), pk2(cs[2], cs[3])};
    if (roleH) {
      *(LAS v2u*)(L + L_X + (tb * 512 + tk) * 2) = xv;
      const float xs[4] = {bf_lo(xv.x), bf_hi(xv.x), bf_lo(xv.y), bf_hi(xv.y)};
      float sum, m2;
      ln_wave_stats(xs, sum, m2);
      if (lane == 0) *(LAS v2u*)(L + M_LN + w * 8) = v2u{__float_as_uint(sum), __float_as_uint(m2)};
    }
  }
  STAMP(17);
  if (roleM || roleQ) asm volatile("s_waitcnt vmcnt(8)" ::: "memory");
  STAMP(18);
  bar();
  STAMP(1);
  if (roleH && w < 2) {
    const float ss = rb(sel4(gemv_tile(L + L_W, L + L_ACT, lane, w), g));
    const float cc = rb(sel4(gemv_tile(L + L_W, L + L_ACT, lane, w + 2), g));
    float mean_r, den_r;
    ln_row(L, g, mean_r, den_r);
    const int col = 32 * (2 * X + J) + 16 * w + n;
    const float xx = bf_lo(*(LAS unsigned short*)(L + L_X + (g * 512 + col) * 2));
    const unsigned hb = __float_as_uint(ln_mod(xx, mean_r, den_r, cc, ss)) >> 16;
    const unsigned d0 = dppu<0x00>(hb) | (dppu<0x55>(hb) << 16), d1 = dppu<0xAA>(hb) | (dppu<0xFF>(hb) << 16);
    if ((n & 3) == 0) ST_SC1((v4u{d0, ta, d1, tb2}), WS_H + (col >> 3) * 128 + (g * 2 + ((n >> 2) & 1)) * 16);
    FENCE();
  }
  if (roleG && w < 4) {
    const f32x4 d = gemv_tile(L + L_W, L + L_ACT, lane, w);
    unsigned o[4];
#pragma unroll
    for (int b = 0; b < 4; ++b) { const unsigned hb = __float_as_uint(rb(d[b])) >> 16; o[b] = nosentu(hb | (dppu<XOR1>(hb) << 16)); }
    if (lane < 16 && (lane & 1) == 0) *(LAS v4u*)(L + L_RED + (8 * w + (lane >> 1)) * 16) = v4u{o[0], o[1], o[2], o[3]};
    FENCE();
  }
  if (roleG) bar();
  STAMP(2);
#define PACE_WAIT() do { if (PACE >= 0) asm volatile("s_waitcnt vmcnt(%0)" ::"i"(PACE < 0 ? 0 : PACE) : "memory"); } while (0)
  if (w == 1) {
    asm volatile("s_waitcnt vmcnt(8)" ::: "memory");
    if (roleG && lane < 32) ST_SC1(lds128(L + L_RED + lane * 16), WS_GATE + (4 * X + (J - J_G)) * 512 + lane * 16);
    if (lane == 0) ST_SC1((v4u{0u, ta, 0u, tb2}), WS_READY + cu * 16);
    FENCE();
  }
  PACE_WAIT(); K_ISSUE(2); PACE_WAIT(); K_ISSUE(3);
  if (w == 0 && roleQ) {
    v4u hv[8];
    for (unsigned spin = 0; spin < SPIN_MAX; ++spin) {
      bool ok = true;
#pragma unroll
      for (int i = 0; i < 8; ++i) { hv[i] = LD_SC1(WS_H + (64 * i + lane) * 16); ok &= (hv[i].y == ta) & (hv[i].w == tb2); }
      if (all_lanes(ok)) break;
      asm volatile("" ::: "memory");
    }
    STAMP(3);
#pragma unroll
    for (int i = 0; i < 8; ++i) { const int gm = 64 * i + lane; *(LAS v2u*)(L + L_ACT + ((gm >> 1) & 3) * RS + act_off(8 * (gm >> 3) + 4 * (gm & 1))) = v2u{hv[i].x, hv[i].z}; }
    asm volatile("s_waitcnt lgkmcnt(0)" ::: "memory");
#pragma unroll
    for (int ct = 0; ct < 4; ++ct) {
      const f32x4 d = gemv_tile(L + L_W, L + L_ACT, lane, ct);
      const unsigned qb = __float_as_uint(rb(sel4(d, g))) >> 16;
      const unsigned d0 = dppu<0x00>(qb) | (dppu<0x55>(qb) << 16), d1 = dppu<0xAA>(qb) | (dppu<0xFF>(qb) << 16);
      if ((n & 3) == 0) ST_SC1((v4u{d0, ta, d1, tb2}), WS_Q + (8 * X + (J - J_Q)) * 1024 + (g * 16 + 4 * ct + (n >> 2)) * 16);
    }
    FENCE();
    STAMP(4);
  }
  PACE_WAIT(); K_ISSUE(4); PACE_WAIT(); K_ISSUE(5);
  if (w == 0) {
    v4u qv[2];
    const bool need_ready = roleB || roleC;
    for (unsigned spin = 0; spin < SPIN_MAX; ++spin) {
      bool ok = true;
#pragma unroll
      for (int i = 0; i < 2; ++i) { const int gm = 64 * i + lane; qv[i] = LD_SC1(WS_Q + (8 * X + (gm >> 4)) * 1024 + ((J >> 3) * 16 + (gm & 15)) * 16); ok &= (qv[i].y == ta) & (qv[i].w == tb2); }
      if (need_ready) {
#pragma unroll
        for (int i = 0; i < 4; ++i) { const v4u rd = LD_SC1(WS_READY + (64 * i + lane) * 16); ok &= (rd.y == ta) & (rd.w == tb2); }
      }
      if (all_lanes(ok)) break;
      asm volatile("" ::: "memory");
    }
#pragma unroll
    for (int i = 0; i < 2; ++i) *(LAS v2u*)(L + L_Q + (64 * i + lane) * 8) = v2u{qv[i].x, qv[i].z};
    asm volatile("s_waitcnt lgkmcnt(0)" ::: "memory");
    if (lane == 0) *(volatile LAS unsigned*)(L + M_FLAG) = 1u;
    if (cu == 8 * J_B && lane == 0) __hip_atomic_store((GAS unsigned long long*)p.ws, E, __ATOMIC_RELAXED, __HIP_MEMORY_SCOPE_AGENT);
    FENCE();
    STAMP(5);
  }
  PACE_WAIT(); K_ISSUE(6); PACE_WAIT(); K_ISSUE(7);
  v4u vf[4][8];
#define V_ISSUE(u) do { unsigned vo_ = lane * 16; asm volatile("" : "+v"(vo_)); _Pragma("unroll") for (int j_ = 0; j_ < 8; ++j_) vf[u][j_] = __builtin_amdgcn_raw_buffer_load_b128(rv, vo_ + (j_ & 3) * 1024, kvbase + (2 * (u) + (j_ >> 2)) * 32768, AUX_NT); FENCE(); } while (0)
  PACE_WAIT(); V_ISSUE(0);
  STAMP(6);
  for (unsigned spin = 0; spin < (SPIN_MAX << 6); ++spin) {
    if (*(volatile LAS unsigned*)(L + M_FLAG) != 0u) break;
    __builtin_amdgcn_s_sleep(2);
  }
  asm volatile("" ::: "memory");
  STAMP(7);
  v4u gt[2] = {v4u{0u, 0u, 0u, 0u}, v4u{0u, 0u, 0u, 0u}};
  if (roleC) {
#pragma unroll
    for (int e = 0; e < 2; ++e) gt[e] = LD_SC1(WS_GATE + ((2 * w + e) * 64 + lane) * 16);
  }
  FENCE();
  const v4u qf0 = lds128(L + L_Q + (n & 3) * 256 + g * 32 + ((n >> 2) & 1) * 16), qf1 = lds128(L + L_Q + (n & 3) * 256 + 128 + g * 32 + ((n >> 2) & 1) * 16);
  float S[8][4];
#define QK(t) do { const f32x4 z_ = {0.f, 0.f, 0.f, 0.f}; \
    const f32x4 d0_ = mfma(kf[t][1], qf1, mfma(kf[t][0], qf0, z_)), d1_ = mfma(kf[t][3], qf1, mfma(kf[t][2], qf0, z_)); \
    _Pragma("unroll") for (int r_ = 0; r_ < 4; ++r_) { \
      auto s_ = __builtin_amdgcn_permlane32_swap(__float_as_uint(d0_[r_]), __float_as_uint(d1_[r_]), false, false); \
      S[t][r_] = __uint_as_float(s_[0]) + dpp<ROR4>(__uint_as_float(s_[1])); } } while (0)
  QK(0); QK(1); FENCE(); V_ISSUE(1);
  QK(2); QK(3); FENCE(); V_ISSUE(2);
  QK(4); QK(5); FENCE(); V_ISSUE(3);
  QK(6); QK(7); FENCE();
  STAMP(8);
  float mx = -__builtin_inff();
#pragma unroll
  for (int t = 0; t < 8; ++t)
#pragma unroll
    for (int r = 0; r < 4; r += 2) {
      const unsigned a = pk2(S[t][r], S[t][r + 1]);
      const unsigned b = pk2(bf_lo(a) * 0.08838834764831845f, bf_hi(a) * 0.08838834764831845f);
      S[t][r] = bf_lo(b); S[t][r + 1] = bf_hi(b);
      mx = fmaxf(mx, fmaxf(S[t][r], S[t][r + 1]));
    }
  mx = x32_max(x16_max(mx));
  float ls = 0.f;
  v4u pf[4];
#pragma unroll
  for (int u = 0; u < 4; ++u) {
    float e[8];
#pragma unroll
    for (int j = 0; j < 8; ++j) { e[j] = __builtin_amdgcn_exp2f((S[2 * u + (j >> 2)][j & 3] - mx) * LOG2E); ls += e[j]; }
    pf[u] = v4u{pk2(e[0], e[1]), pk2(e[2], e[3]), pk2(e[4], e[5]), pk2(e[6], e[7])};
  }
  ls = x32_sum(x16_sum(ls));
  if (lane < 4) *(LAS v2u*)(L + M_ML + w * 32 + lane * 8) = v2u{__float_as_uint(mx), __float_as_uint(ls)};
  if (roleC) {
    if (!all_lanes(umax4(gt[0]) != SENT && umax4(gt[1]) != SENT)) {
      for (unsigned spin = 0; spin < SPIN_MAX; ++spin) {
        asm volatile("" ::: "memory");
#pragma unroll
        for (int e = 0; e < 2; ++e) gt[e] = LD_SC1(WS_GATE + ((2 * w + e) * 64 + lane) * 16);
        if (all_lanes(umax4(gt[0]) != SENT && umax4(gt[1]) != SENT)) break;
      }
      asm volatile("s_waitcnt vmcnt(0)" ::: "memory");
    }
#pragma unroll
    for (int e = 0; e < 2; ++e) {
      const int gm = (2 * w + e) * 64 + lane;
#pragma unroll
      for (int b = 0; b < 4; ++b) *(LAS unsigned*)(L + L_GATE + b * 4096 + gm * 4) = gt[e][b];
    }
  }
  FENCE();
  STAMP(9);
  f32x4 oa[8];
#pragma unroll
  for (int d = 0; d < 8; ++d) oa[d] = f32x4{0.f, 0.f, 0.f, 0.f};
#pragma unroll
  for (int u = 0; u < 4; ++u) {
#pragma unroll
    for (int d = 0; d < 8; ++d) {
      const unsigned sel = (d & 1) ? 0x07060302u : 0x05040100u;
      v4u a;
#pragma unroll
      for (int i = 0; i < 4; ++i) a[i] = __builtin_amdgcn_perm(vf[u][2 * i + 1][d >> 1], vf[u][2 * i][d >> 1], sel);
      oa[d] = mfma(a, pf[u], oa[d]);
    }
    FENCE();
  }
  STAMP(10);
  int tid2 = threadIdx.x; asm volatile("" : "+v"(tid2));
#define tid tid2
  const int lane2 = tid2 & 63;
#define lane lane2
  const int g2 = lane2 >> 4, n2 = lane2 & 15;
#define g g2
#define n n2
  const int tb = tid2 >> 7, tk = 4 * (tid2 & 127);
  if (n < 4) {
#pragma unroll
    for (int r = 0; r < 4; ++r) {
      *(LAS f32x4*)(L + L_MRG + w * MRG_W + n * MRG_H + r * 128 + g * 32) = f32x4{oa[0][r], oa[1][r], oa[2][r], oa[3][r]};
      *(LAS f32x4*)(L + L_MRG + w * MRG_W + n * MRG_H + r * 128 + g * 32 + 16) = f32x4{oa[4][r], oa[5][r], oa[6][r], oa[7][r]};
    }
  }
  bar();
  if (w < 2) {
    const int h = tid >> 5, pos = 4 * (tid & 31);
    float mw[8], M = -__builtin_inff();
#pragma unroll
    for (int ww = 0; ww < 8; ++ww) { mw[ww] = *(LAS float*)(L + M_ML + ww * 32 + h * 8); M = fmaxf(M, mw[ww]); }
    f32x4 o = {0.f, 0.f, 0.f, 0.f}; float Ls = 0.f;
#pragma unroll
    for (int ww = 0; ww < 8; ++ww) {
      const float f = __builtin_amdgcn_exp2f((mw[ww] - M) * LOG2E);
      Ls += f * *(LAS float*)(L + M_ML + ww * 32 + h * 8 + 4);
      o += f * *(LAS f32x4*)(L + L_MRG + ww * MRG_W + h * MRG_H + pos * 4);
    }
    ST_SC1((v4u{nosent(o.x), nosent(o.y), nosent(o.z), nosent(o.w)}), WS_PART + cu * 2048 + h * 512 + pos * 4);
    if ((tid & 31) == 0) __builtin_amdgcn_raw_buffer_store_b64((v2u{nosent(M), nosent(Ls)}), rws, WS_STAT + cu * 32 + h * 8, 0, AUX_SC1);
  }
  STAMP(11);
  if (!(roleB || roleC)) {
    return;
  }
  asm volatile("s_barrier" ::: "memory");
  if (roleB) {
    {
      const int r0 = 64 * w + (lane >> 3);
      const GAS unsigned char* s = (const GAS unsigned char*)p.wo + ((size_t)(512 * X + r0) * 512 + 64 * (J - J_B) + 8 * img_chunk(lane, r0)) * 2;
#pragma unroll
      for (int e = 0; e < 8; ++e) glds16((const void*)(s + (size_t)e * 8 * 512 * 2), lds0 + L_W + w * 8192 + e * 1024);
    }
    const int bb = w >> 1, hf = lane >> 5;
    v4u po[2][4]; v2u st[2];
    for (unsigned spin = 0; spin < SPIN_MAX; ++spin) {
      unsigned m = 0;
#pragma unroll
      for (int hh = 0; hh < 2; ++hh) {
        const int lh = 2 * (w & 1) + hh;
#pragma unroll
        for (int i = 0; i < 4; ++i) { po[hh][i] = LD_SC1(WS_PART + ((8 * bb + 2 * i + hf) * 8 + X) * 2048 + lh * 512 + (lane & 31) * 16); m = max(m, umax4(po[hh][i])); }
        st[hh] = __builtin_amdgcn_raw_buffer_load_b64(rws, WS_STAT + ((8 * bb + 2 * (lane & 3) + hf) * 8 + X) * 32 + lh * 8, 0, AUX_SC1);
        m = max(m, max(st[hh].x, st[hh].y));
      }
      if (all_lanes(m != SENT)) break;
      asm volatile("" ::: "memory");
    }
    STAMP(12);
    asm volatile("s_waitcnt vmcnt(0)" ::: "memory");
    v4u bo[2][4];
#pragma unroll
    for (int ct = 0; ct < 4; ++ct) { lptr wp = L + L_W + w * 8192 + frag_base(lane, ct); bo[0][ct] = w_frag(wp, 0); bo[1][ct] = w_frag(wp, 1); }
    STAMP(19);
#pragma unroll
    for (int hh = 0; hh < 2; ++hh) {
      const float ms = __uint_as_float(st[hh].x);
      float M = fmaxf(ms, dpp<XOR1>(ms)); M = fmaxf(M, dpp<XOR2>(M)); M = x32_max(M);
      const float f = __builtin_amdgcn_exp2f((ms - M) * LOG2E);
      float Ls = f * __uint_as_float(st[hh].y); Ls += dpp<XOR1>(Ls); Ls += dpp<XOR2>(Ls); Ls = x32_sum(Ls);
      const float wt = f / Ls;
      const float w0 = dpp<0x00>(wt), w1 = dpp<0x55>(wt), w2 = dpp<0xAA>(wt), w3 = dpp<0xFF>(wt);
      float o[4];
#pragma unroll
      for (int e = 0; e < 4; ++e)
        o[e] = x32_sum(w0 * __uint_as_float(po[hh][0][e]) + w1 * __uint_as_float(po[hh][1][e]) + w2 * __uint_as_float(po[hh][2][e]) + w3 * __uint_as_float(po[hh][3][e]));
      const int pi = 4 * (lane & 31), dim = 32 * ((pi >> 3) & 3) + 8 * (pi >> 5) + (pi & 7);
      if (lane < 32) *(LAS v2u*)(L + L_ACT + bb * RS + act_off(128 * (2 * (w & 1) + hh) + dim)) = v2u{pk2(o[0], o[1]), pk2(o[2], o[3])};
    }
    STAMP(20);
    bar();
    STAMP(21);
    {
      lptr ap = L + L_ACT + (lane & 3) * RS + g * 16 + (2 * w) * 64;
      const v4u a0 = lds128(ap), a1 = lds128(ap + 64);
#pragma unroll
      for (int ct = 0; ct < 4; ++ct) {
        f32x4 d = mfma(a0, bo[0][ct], f32x4{0.f, 0.f, 0.f, 0.f});
        d = mfma(a1, bo[1][ct], d);
        *(LAS float*)(L + L_RED + w * 1024 + g * 256 + (16 * ct + n) * 4) = sel4(d, g);
      }
    }
    bar();
    STAMP(22);
    if (tid < 64) {
      f32x4 s = {0.f, 0.f, 0.f, 0.f};
#pragma unroll
      for (int ww = 0; ww < 8; ++ww) s += *(LAS f32x4*)(L + L_RED + ww * 1024 + tid * 16);
      ST_SC1((v4u{nosent(s.x), nosent(s.y), nosent(s.z), nosent(s.w)}), WS_ATT + ((X * 4 + g) * 512 + 64 * (J - J_B) + 4 * n) * 4);
    }
    STAMP(13);
    return;
  }
  const int pc = 7 * X + (J - J_C);
  {
    const int r0 = 64 * w + (lane >> 3), ch = img_chunk(lane, r0);
    const GAS unsigned char* s = (const GAS unsigned char*)((ch >> 2) ? p.wu : p.wg) + ((size_t)r0 * 1792 + 32 * pc + 8 * (ch & 3)) * 2;
#pragma unroll
    for (int e = 0; e < 8; ++e) glds16((const void*)(s + (size_t)e * 8 * 1792 * 2), lds0 + L_W + w * 8192 + e * 1024);
    const int rd = lane >> 3;
    const GAS unsigned char* sd = (const GAS unsigned char*)p.wd + ((size_t)(32 * pc + rd) * 512 + 64 * w + 8 * img_chunk(lane, rd)) * 2;
#pragma unroll
    for (int e = 0; e < 4; ++e) glds16((const void*)(sd + (size_t)e * 8 * 512 * 2), lds0 + L_WD + w * 4096 + e * 1024);
  }
  const v2u xr = *(const GAS v2u*)((const GAS unsigned char*)p.x + (tb * 512 + tk) * 2);
  STAMP(12); STAMP(13);
  {
    v4u at[8];
    for (unsigned spin = 0; spin < SPIN_MAX; ++spin) {
      unsigned m = 0;
#pragma unroll
      for (int i = 0; i < 8; ++i) { at[i] = LD_SC1(WS_ATT + ((i * 4 + tb) * 512 + tk) * 4); m = max(m, umax4(at[i])); }
      if (all_lanes(m != SENT)) break;
      asm volatile("" ::: "memory");
    }
    STAMP(14);
    asm volatile("s_waitcnt vmcnt(0)" ::: "memory");
    v4u bg[2][4], bd[4];
#pragma unroll
    for (int ct = 0; ct < 4; ++ct) { lptr wp = L + L_W + w * 8192 + frag_base(lane, ct); bg[0][ct] = w_frag(wp, 0); bg[1][ct] = w_frag(wp, 1); }
    {
      const int q = (lane >> 2) & 3;
#pragma unroll
      for (int kp = 0; kp < 4; ++kp) {
        const int ra = 8 * kp + q, rb_ = ra + 4;
        bd[kp] = tr_frag(L + L_WD + w * 4096 + ra * 128 + ((g ^ ((ra >> 1) & 3)) * 32) + (lane & 3) * 8, L + L_WD + w * 4096 + rb_ * 128 + ((g ^ ((rb_ >> 1) & 3)) * 32) + (lane & 3) * 8);
      }
    }
    STAMP(19);
    const v2u ga = *(LAS v2u*)(L + L_GATE + tb * 4096 + tk * 2), sb = *(LAS v2u*)(L + L_GATE + tb * 4096 + 1024 + tk * 2), cb = *(LAS v2u*)(L + L_GATE + tb * 4096 + 2048 + tk * 2);
    const float gav[4] = {bf_lo(ga.x), bf_hi(ga.x), bf_lo(ga.y), bf_hi(ga.y)}, sbv[4] = {bf_lo(sb.x), bf_hi(sb.x), bf_lo(sb.y), bf_hi(sb.y)},
                cbv[4] = {bf_lo(cb.x), bf_hi(cb.x), bf_lo(cb.y), bf_hi(cb.y)}, xs[4] = {bf_lo(xr.x), bf_hi(xr.x), bf_lo(xr.y), bf_hi(xr.y)};
    float x2[4];
#pragma unroll
    for (int e = 0; e < 4; ++e) {
      float a = 0.f;
#pragma unroll
      for (int i = 0; i < 8; ++i) a += __uint_as_float(at[i][e]);
      x2[e] = rb(xs[e] + rb(gav[e] * rb(a)));
    }
    *(LAS v2u*)(L + L_X + (tb * 512 + tk) * 2) = v2u{pk2(x2[0], x2[1]), pk2(x2[2], x2[3])};
    float sum, m2;
    ln_wave_stats(x2, sum, m2);
    if (lane == 0) *(LAS v2u*)(L + M_LN + w * 8) = v2u{__float_as_uint(sum), __float_as_uint(m2)};
    bar();
    STAMP(20);
    float mean_r, den_r;
    ln_row(L, tb, mean_r, den_r);
    float h2[4];
#pragma unroll
    for (int e = 0; e < 4; ++e) h2[e] = ln_mod(x2[e], mean_r, den_r, cbv[e], sbv[e]);
    *(LAS v2u*)(L + L_ACT + tb * RS + act_off(tk)) = v2u{pk2(h2[0], h2[1]), pk2(h2[2], h2[3])};
    bar();
    STAMP(21);
    {
      lptr ap = L + L_ACT + (lane & 3) * RS + g * 16 + (2 * w) * 64;
      const v4u a0 = lds128(ap), a1 = lds128(ap + 64);
#pragma unroll
      for (int ct = 0; ct < 4; ++ct) {
        f32x4 d = mfma(a0, bg[0][ct], f32x4{0.f, 0.f, 0.f, 0.f});
        d = mfma(a1, bg[1][ct], d);
        *(LAS float*)(L + L_RED + w * 1024 + g * 256 + (16 * ct + n) * 4) = sel4(d, g);
      }
    }
    bar();
    STAMP(22);
    if (w < 2) {
      const int b = 2 * w + (lane >> 5), un = lane & 31;
      float s = 0.f, up = 0.f;
#pragma unroll
      for (int ww = 0; ww < 8; ++ww) { s += *(LAS float*)(L + L_RED + ww * 1024 + b * 256 + un * 4); up += *(LAS float*)(L + L_RED + ww * 1024 + b * 256 + 128 + un * 4); }
      s = rb(s); up = rb(up);
      const float u = 0.7978845608028654f * (s + 0.044715f * s * s * s);
      const float ge = rb(s / (1.f + __builtin_amdgcn_exp2f(-2.f * LOG2E * u)));
      *(LAS unsigned short*)(L + M_HID + b * 64 + un * 2) = (unsigned short)(__float_as_uint(rb(ge * up)) >> 16);
    }
    bar();
    STAMP(23);
    f32x4 d = {0.f, 0.f, 0.f, 0.f};
#pragma unroll
    for (int kp = 0; kp < 4; ++kp) {
      v4u a4 = lds128(L + M_HID + (n & 3) * 64 + kp * 16);
      if ((n >> 2) != g) a4 = v4u{0u, 0u, 0u, 0u};
      d = mfma(a4, bd[kp], d);
    }
    ST_SC1((v4u{nosent(d[0]), nosent(d[1]), nosent(d[2]), nosent(d[3])}), WS_DOWN + pc * 8192 + (64 * w + lane) * 16);
  }
  STAMP(15);
  if (J < J_F_END) {
    const int f = 4 * X + (J - J_C);
    if (w < 7) {
      v4u dv[2];
      for (unsigned spin = 0; spin < SPIN_MAX; ++spin) {
#pragma unroll
        for (int i = 0; i < 2; ++i) dv[i] = LD_SC1(WS_DOWN + (28 * i + 4 * w + g) * 8192 + (16 * f + n) * 16);
        if (all_lanes(max(umax4(dv[0]), umax4(dv[1])) != SENT)) break;
        asm volatile("" ::: "memory");
      }
      f32x4 s;
#pragma unroll
      for (int b = 0; b < 4; ++b) s[b] = x32_sum(x16_sum(__uint_as_float(dv[0][b]) + __uint_as_float(dv[1][b])));
      if (lane < 16) *(LAS f32x4*)(L + L_RED + w * 256 + lane * 16) = s;
    }
    bar();
    if (tid < 64) {
      const int b = tid >> 4, col = 16 * f + n;
      float s = 0.f;
#pragma unroll
      for (int ww = 0; ww < 7; ++ww) s += *(LAS float*)(L + L_RED + ww * 256 + n * 16 + b * 4);
      const float y = rb(s);
      const float gb = bf_lo(*(LAS unsigned short*)(L + L_GATE + b * 4096 + 3072 + col * 2));
      const float x2 = bf_lo(*(LAS unsigned short*)(L + L_X + (b * 512 + col) * 2));
      const unsigned ob = pk2(x2 + rb(gb * y), 0.f) & 0xffffu;
      const unsigned o2 = ob | (dppu<XOR1>(ob) << 16);
      if ((n & 1) == 0) *(GAS unsigned*)((GAS unsigned char*)p.out + (b * 512 + col) * 2) = o2;
    }
  }
#undef tid
#undef lane
#undef g
#undef n
}
}

extern "C" void kernel_launch(void* const* d_in, const int* in_sizes, int n_in, void* d_out, int out_size, void* d_ws, size_t ws_size, hipStream_t stream) {
  (void)in_sizes; (void)n_in; (void)out_size; (void)ws_size;
  Params p;
  p.x = d_in[0]; p.k = d_in[1]; p.v = d_in[2]; p.wq = d_in[3]; p.wo = d_in[4];
  p.wg = d_in[6]; p.wu = d_in[7]; p.wd = d_in[8]; p.c = d_in[9]; p.wm = d_in[10];
  p.out = d_out; p.ws = d_ws;
  hipLaunchKernelGGL(adaln_block_kernel, dim3(NCU), dim3(NTHR), 0, stream, p);
}
```

```cpp
#include <hip/hip_runtime.h>
#include <hip/hip_bf16.h>
#include <stdint.h>

#define GAS __attribute__((address_space(1)))
#define LAS __attribute__((address_space(3)))
typedef unsigned v4u __attribute__((ext_vector_type(4)));
typedef unsigned v2u __attribute__((ext_vector_type(2)));
typedef float f32x4 __attribute__((ext_vector_type(4)));
typedef short s16x4 __attribute__((ext_vector_type(4)));
typedef __bf16 bf16x8 __attribute__((ext_vector_type(8)));
typedef __bf16 bf16x2 __attribute__((ext_vector_type(2)));
typedef LAS unsigned char* lptr;

#define PACE 4
#define STAMPS 0

namespace {
constexpr int NCU = 256, NTHR = 512;
constexpr unsigned KV_BYTES = 4u * 8u * 8192u * 128u * 2u;
constexpr unsigned WS_EPOCH = 0, WS_READY = 4096, WS_H = 8192, WS_Q = 16384, WS_GATE = WS_Q + 65536, WS_STAT = WS_GATE + 16384, WS_ATT = WS_STAT + 8192,
                   WS_PART = WS_ATT + 65536, WS_DOWN = WS_PART + 256 * 2048, WS_END = WS_DOWN + 56 * 8192, WS_STAMP = WS_END + 65536;
constexpr int RS = 1088;
constexpr int L_W = 0, L_WD = 65536, L_MRG = 98304, L_GATE = L_MRG + 17408, L_ACT = L_GATE + 16384, L_X = L_ACT + 4352, L_Q = L_X + 4096, L_RED = L_Q + 1024,
              L_MISC = L_RED + 8192, L_BYTES = L_MISC + 1024;
constexpr int MRG_H = 528, MRG_W = 4 * MRG_H;
constexpr int M_LN = L_MISC, M_ML = L_MISC + 64, M_HID = L_MISC + 320, M_FLAG = L_MISC + 576;
constexpr unsigned SENT = 0xFFFFFFFFu;
constexpr unsigned SPIN_MAX = 1u << 15;
constexpr int AUX_NT = 2, AUX_SC1 = 16;
constexpr float LOG2E = 1.4426950408889634f;
constexpr int J_H = 0, J_G = 2, J_Q = 6, J_B = 14, J_C = 22, J_F_END = 26, J_C_END = 29;

struct Params { const void* x; const void* k; const void* v; const void* wq; const void* wo; const void* wg; const void* wu; const void* wd; const void* c; const void* wm; void* out; void* ws; };

__device__ __forceinline__ float bf_lo(unsigned u) { return __builtin_bit_cast(float, u << 16); }
__device__ __forceinline__ float bf_hi(unsigned u) { return __builtin_bit_cast(float, u & 0xffff0000u); }
__device__ __forceinline__ unsigned pk2(float a, float b) { bf16x2 r = {(__bf16)a, (__bf16)b}; return __builtin_bit_cast(unsigned, r); }
__device__ __forceinline__ float rb(float a) { return bf_lo(pk2(a, a)); }
template <int CTRL> __device__ __forceinline__ float dpp(float x) { return __builtin_bit_cast(float, __builtin_amdgcn_mov_dpp(__builtin_bit_cast(int, x), CTRL, 0xf, 0xf, true)); }
template <int CTRL> __device__ __forceinline__ unsigned dppu(unsigned x) { return (unsigned)__builtin_amdgcn_mov_dpp((int)x, CTRL, 0xf, 0xf, true); }
constexpr int XOR1 = 0xB1, XOR2 = 0x4E, HMIR = 0x141, ROR4 = 0x124, ROR8 = 0x128;
__device__ __forceinline__ float x16_sum(float x) { auto s = __builtin_amdgcn_permlane16_swap(__float_as_uint(x), __float_as_uint(x), false, false); return __uint_as_float(s[0]) + __uint_as_float(s[1]); }
__device__ __forceinline__ float x32_sum(float x) { auto s = __builtin_amdgcn_permlane32_swap(__float_as_uint(x), __float_as_uint(x), false, false); return __uint_as_float(s[0]) + __uint_as_float(s[1]); }
__device__ __forceinline__ float x16_max(float x) { auto s = __builtin_amdgcn_permlane16_swap(__float_as_uint(x), __float_as_uint(x), false, false); return fmaxf(__uint_as_float(s[0]), __uint_as_float(s[1])); }
__device__ __forceinline__ float x32_max(float x) { auto s = __builtin_amdgcn_permlane32_swap(__float_as_uint(x), __float_as_uint(x), false, false); return fmaxf(__uint_as_float(s[0]), __uint_as_float(s[1])); }
__device__ __forceinline__ float wave_sum(float x) { x += dpp<XOR1>(x); x += dpp<XOR2>(x); x += dpp<HMIR>(x); x += dpp<ROR8>(x); return x32_sum(x16_sum(x)); }
__device__ __forceinline__ f32x4 mfma(v4u a, v4u b, f32x4 c) { return __builtin_amdgcn_mfma_f32_16x16x32_bf16(__builtin_bit_cast(bf16x8, a), __builtin_bit_cast(bf16x8, b), c, 0, 0, 0); }
__device__ __forceinline__ v2u tr_read(lptr p) { s16x4 r = __builtin_amdgcn_ds_read_tr16_b64_v4i16((LAS s16x4*)p); return __builtin_bit_cast(v2u, r); }
__device__ __forceinline__ v4u tr_frag(lptr p0, lptr p1) { v2u a = tr_read(p0), b = tr_read(p1); return v4u{a.x, a.y, b.x, b.y}; }
__device__ __forceinline__ v4u lds128(lptr p) { return *(LAS v4u*)p; }
__device__ __forceinline__ void bar() { asm volatile("s_waitcnt lgkmcnt(0)\n\ts_barrier" ::: "memory"); }
__device__ __forceinline__ void glds16(const void* gsrc, unsigned lds_dst) {
  unsigned keep;
  asm volatile("s_mov_b32 %0, m0\n\ts_mov_b32 m0, %2\n\ts_nop 0\n\tglobal_load_lds_dwordx4 %1, off nt\n\ts_mov_b32 m0, %0" : "=&s"(keep) : "v"(gsrc), "s"(lds_dst) : "memory");
}
__device__ __forceinline__ void glds16_keep(const void* gsrc, unsigned lds_dst) {
  unsigned keep;
  asm volatile("s_mov_b32 %0, m0\n\ts_mov_b32 m0, %2\n\ts_nop 0\n\tglobal_load_lds_dwordx4 %1, off\n\ts_mov_b32 m0, %0" : "=&s"(keep) : "v"(gsrc), "s"(lds_dst) : "memory");
}
__device__ __forceinline__ unsigned nosent(float f) { unsigned u = __float_as_uint(f); return u == SENT ? SENT - 1u : u; }
__device__ __forceinline__ unsigned nosentu(unsigned u) { return u == SENT ? SENT - 1u : u; }
__device__ __forceinline__ unsigned umax4(v4u a) { return max(max(a.x, a.y), max(a.z, a.w)); }
__device__ __forceinline__ bool all_lanes(bool ok) { return __builtin_amdgcn_ballot_w64(!ok) == 0ull; }
__device__ __forceinline__ float sel4(f32x4 d, int g) { return g == 0 ? d[0] : g == 1 ? d[1] : g == 2 ? d[2] : d[3]; }
#define RSRC(p, n) __builtin_amdgcn_make_buffer_rsrc((void*)(p), 0, (int)(n), 0x00020000)
#define LD_SC1(off) __builtin_amdgcn_raw_buffer_load_b128(rws, (off), 0, AUX_SC1)
#define ST_SC1(v, off) __builtin_amdgcn_raw_buffer_store_b128((v), rws, (off), 0, AUX_SC1)
#define FENCE() __builtin_amdgcn_sched_barrier(0)
#define STAMP(i) do { } while (0)

__device__ __forceinline__ int img_chunk(int lane, int row) { return ((((lane & 7) >> 1) ^ ((row >> 1) & 3)) << 1) | (lane & 1); }
__device__ __forceinline__ int frag_base(int lane, int ct) { const int g = lane >> 4, q = (lane >> 2) & 3; return (4 * g + q) * 128 + ((ct ^ ((2 * g + (q >> 1)) & 3)) * 32) + (lane & 3) * 8; }
__device__ __forceinline__ v4u w_frag(lptr base, int s) { return tr_frag(base + s * 4096, base + s * 4096 + 2048); }
__device__ __forceinline__ int act_off(int k) { return (k >> 5) * 64 + ((k >> 2) & 3) * 16 + ((k >> 4) & 1) * 8; }
__device__ __forceinline__ f32x4 gemv_tile(lptr W, lptr act, int lane, int ct) {
  lptr ap = act + (lane & 3) * RS + (lane >> 4) * 16;
  lptr wp = W + frag_base(lane, ct);
  f32x4 a0 = {0.f, 0.f, 0.f, 0.f}, a1 = {0.f, 0.f, 0.f, 0.f};
#pragma unroll
  for (int s = 0; s < 16; s += 2) {
    a0 = mfma(lds128(ap + s * 64), w_frag(wp, s), a0);
    a1 = mfma(lds128(ap + s * 64 + 64), w_frag(wp, s + 1), a1);
  }
  return a0 + a1;
}
__device__ __forceinline__ void ln_wave_stats(const float (&v)[4], float& sum, float& m2) {
  sum = wave_sum((v[0] + v[1]) + (v[2] + v[3]));
  const float mw = sum * (1.f / 256.f);
  const float d0 = v[0] - mw, d1 = v[1] - mw, d2 = v[2] - mw, d3 = v[3] - mw;
  m2 = wave_sum((d0 * d0 + d1 * d1) + (d2 * d2 + d3 * d3));
}
__device__ __forceinline__ void ln_row(lptr L, int b, float& mean_r, float& den_r) {
  const f32x4 s = *(LAS f32x4*)(L + M_LN + b * 16);
  const float dm = (s.x - s.z) * (1.f / 256.f);
  const float mean = (s.x + s.z) * (1.f / 512.f);
  const float var = (s.y + s.w + dm * dm * 128.f) * (1.f / 512.f);
  mean_r = rb(mean);
  den_r = rb(__builtin_sqrtf(rb(rb(var) + 1e-5f)));
}
__device__ __forceinline__ float ln_mod(float x, float mean_r, float den_r, float cc, float ss) {
  const float ln = rb(rb(x - mean_r) / den_r);
  return rb(rb(ln * rb(1.f + cc)) + ss);
}

__global__ void __launch_bounds__(NTHR, 2) adaln_block_kernel(Params p) {
  __shared__ __attribute__((aligned(16))) unsigned char smem[L_BYTES + (STAMPS ? 1024 : 0)];
  lptr L = (lptr)smem;
  const unsigned lds0 = (unsigned)(uintptr_t)L;
  const int tid = threadIdx.x, lane = tid & 63, w = __builtin_amdgcn_readfirstlane(tid >> 6);
  const int cu = blockIdx.x, X = cu & 7, J = cu >> 3;
  const int g = lane >> 4, n = lane & 15;
  const auto rws = RSRC(p.ws, WS_END);
  const auto rk = RSRC(p.k, KV_BYTES);
  const auto rv = RSRC(p.v, KV_BYTES);
  const bool roleH = J < J_G, roleG = J >= J_G && J < J_Q, roleM = J < J_Q, roleQ = J >= J_Q && J < J_B, roleB = J >= J_B && J < J_C, roleC = J >= J_C && J < J_C_END;
  STAMP(0);
  if (w == 2 && J < 24) {
    unsigned long long pc;
    asm volatile("s_getpc_b64 %0" : "=s"(pc));
    const unsigned long long base = (pc & ~127ull) + (unsigned long long)(J * 1024 + lane * 16);
    glds16_keep((const void*)base, lds0 + L_RED + 4096);
  }
  const unsigned long long ep = __hip_atomic_load((GAS unsigned long long*)p.ws, __ATOMIC_RELAXED, __HIP_MEMORY_SCOPE_AGENT);
  v2u cv = {0u, 0u}, xv = {0u, 0u};
  if (roleM) {
    cv = *(const GAS v2u*)((const GAS unsigned char*)p.c + ((tid >> 7) * 512 + 4 * (tid & 127)) * 2);
    if (roleH) xv = *(const GAS v2u*)((const GAS unsigned char*)p.x + ((tid >> 7) * 512 + 4 * (tid & 127)) * 2);
  }
  if (roleM || roleQ) {
    const int r0 = 64 * w + (lane >> 3), ch = img_chunk(lane, r0);
    const GAS unsigned char* s;
    size_t rstride;
    if (roleH) { s = (const GAS unsigned char*)p.wm + ((size_t)r0 * 3072 + (ch >> 2) * 512 + 32 * (2 * X + J) + 8 * (ch & 3)) * 2; rstride = 3072 * 2; }
    else if (roleG) { s = (const GAS unsigned char*)p.wm + ((size_t)r0 * 3072 + 1024 + 64 * (4 * X + (J - J_G)) + 8 * ch) * 2; rstride = 3072 * 2; }
    else { s = (const GAS unsigned char*)p.wq + ((size_t)r0 * 4096 + 64 * (8 * X + (J - J_Q)) + 8 * ch) * 2; rstride = 4096 * 2; }
#pragma unroll
    for (int e = 0; e < 8; ++e) glds16((const void*)(s + (size_t)e * 8 * rstride), lds0 + L_W + w * 8192 + e * 1024);
    asm volatile("s_barrier" ::: "memory");
  }
  if (w == 1) {
    const v4u sent = {SENT, SENT, SENT, SENT};
    ST_SC1(sent, WS_PART + cu * 2048 + lane * 16);
    ST_SC1(sent, WS_PART + cu * 2048 + 1024 + lane * 16);
    if (lane < 2) ST_SC1(sent, WS_STAT + cu * 32 + lane * 16);
    if (roleB) ST_SC1(sent, WS_ATT + ((X * 4 + g) * 512 + 64 * (J - J_B) + 4 * n) * 4);
    if (roleG && lane < 32) ST_SC1(sent, WS_GATE + (4 * X + (J - J_G)) * 512 + lane * 16);
    if (roleC) {
#pragma unroll
      for (int e = 0; e < 8; ++e) ST_SC1(sent, WS_DOWN + (7 * X + (J - J_C)) * 8192 + e * 1024 + lane * 16);
    }
  }
  FENCE();
  const unsigned kvbase = (((unsigned)(J >> 3) * 8u + (unsigned)X) * 8192u + (unsigned)(J & 7) * 1024u) * 256u + (unsigned)w * 4096u;
  const unsigned koff = (4 * (lane & 3) + ((lane >> 2) & 1)) * 256 + (lane >> 3) * 16;
  v4u kf[8][4];
#define K_ISSUE(t) do { unsigned ko_ = koff; asm volatile("" : "+v"(ko_)); _Pragma("unroll") for (int q_ = 0; q_ < 4; ++q_) kf[t][q_] = __builtin_amdgcn_raw_buffer_load_b128(rk, ko_ + (q_ >> 1) * 512 + (q_ & 1) * 128, kvbase + (t) * 32768, AUX_NT); FENCE(); } while (0)
  K_ISSUE(0); K_ISSUE(1);
  if (tid == 0) *(LAS unsigned*)(L + M_FLAG) = 0u;
  const unsigned long long E = ep + 1ull;
  const unsigned ta = __builtin_amdgcn_readfirstlane((unsigned)E), tb2 = __builtin_amdgcn_readfirstlane(((unsigned)E * 0x9E3779B1u) ^ (unsigned)(E >> 32));
  if (roleM) {
    const int tb = tid >> 7, tk = 4 * (tid & 127);
    float cs[4] = {bf_lo(cv.x), bf_hi(cv.x), bf_lo(cv.y), bf_hi(cv.y)};
#pragma unroll
    for (int e = 0; e < 4; ++e) cs[e] = cs[e] / (1.f + __expf(-cs[e]));
    *(LAS v2u*)(L + L_ACT + tb * RS + act_off(tk)) = v2u{pk2(cs[0], cs[1]), pk2(cs[2], cs[3])};
    if (roleH) {
      *(LAS v2u*)(L + L_X + (tb * 512 + tk) * 2) = xv;
      const float xs[4] = {bf_lo(xv.x), bf_hi(xv.x), bf_lo(xv.y), bf_hi(xv.y)};
      float sum, m2;
      ln_wave_stats(xs, sum, m2);
      if (lane == 0) *(LAS v2u*)(L + M_LN + w * 8) = v2u{__float_as_uint(sum), __float_as_uint(m2)};
    }
  }
  STAMP(17);
  if (roleM || roleQ) asm volatile("s_waitcnt vmcnt(8)" ::: "memory");
  STAMP(18);
  bar();
  STAMP(1);
  if (roleH && w < 2) {
    const float ss = rb(sel4(gemv_tile(L + L_W, L + L_ACT, lane, w), g));
    const float cc = rb(sel4(gemv_tile(L + L_W, L + L_ACT, lane, w + 2), g));
    float mean_r, den_r;
    ln_row(L, g, mean_r, den_r);
    const int col = 32 * (2 * X + J) + 16 * w + n;
    const float xx = bf_lo(*(LAS unsigned short*)(L + L_X + (g * 512 + col) * 2));
    const unsigned hb = __float_as_uint(ln_mod(xx, mean_r, den_r, cc, ss)) >> 16;
    const unsigned d0 = dppu<0x00>(hb) | (dppu<0x55>(hb) << 16), d1 = dppu<0xAA>(hb) | (dppu<0xFF>(hb) << 16);
    if ((n & 3) == 0) ST_SC1((v4u{d0, ta, d1, tb2}), WS_H + (col >> 3) * 128 + (g * 2 + ((n >> 2) & 1)) * 16);
    FENCE();
  }
  if (roleG && w < 4) {
    const f32x4 d = gemv_tile(L + L_W, L + L_ACT, lane, w);
    unsigned o[4];
#pragma unroll
    for (int b = 0; b < 4; ++b) { const unsigned hb = __float_as_uint(rb(d[b])) >> 16; o[b] = nosentu(hb | (dppu<XOR1>(hb) << 16)); }
    if (lane < 16 && (lane & 1) == 0) *(LAS v4u*)(L + L_RED + (8 * w + (lane >> 1)) * 16) = v4u{o[0], o[1], o[2], o[3]};
    FENCE();
  }
  if (roleG) bar();
  STAMP(2);
#define PACE_WAIT() do { if (PACE >= 0) asm volatile("s_waitcnt vmcnt(%0)" ::"i"(PACE < 0 ? 0 : PACE) : "memory"); } while (0)
  if (w == 1) {
    asm volatile("s_waitcnt vmcnt(8)" ::: "memory");
    if (roleG && lane < 32) ST_SC1(lds128(L + L_RED + lane * 16), WS_GATE + (4 * X + (J - J_G)) * 512 + lane * 16);
    if (lane == 0) ST_SC1((v4u{0u, ta, 0u, tb2}), WS_READY + cu * 16);
    FENCE();
  }
  PACE_WAIT(); K_ISSUE(2); PACE_WAIT(); K_ISSUE(3);
  if (w == 0 && roleQ) {
    v4u hv[8];
    for (unsigned spin = 0; spin < SPIN_MAX; ++spin) {
      bool ok = true;
#pragma unroll
      for (int i = 0; i < 8; ++i) { hv[i] = LD_SC1(WS_H + (64 * i + lane) * 16); ok &= (hv[i].y == ta) & (hv[i].w == tb2); }
      if (all_lanes(ok)) break;
      asm volatile("" ::: "memory");
    }
    STAMP(3);
#pragma unroll
    for (int i = 0; i < 8; ++i) { const int gm = 64 * i + lane; *(LAS v2u*)(L + L_ACT + ((gm >> 1) & 3) * RS + act_off(8 * (gm >> 3) + 4 * (gm & 1))) = v2u{hv[i].x, hv[i].z}; }
    asm volatile("s_waitcnt lgkmcnt(0)" ::: "memory");
#pragma unroll
    for (int ct = 0; ct < 4; ++ct) {
      const f32x4 d = gemv_tile(L + L_W, L + L_ACT, lane, ct);
      const unsigned qb = __float_as_uint(rb(sel4(d, g))) >> 16;
      const unsigned d0 = dppu<0x00>(qb) | (dppu<0x55>(qb) << 16), d1 = dppu<0xAA>(qb) | (dppu<0xFF>(qb) << 16);
      if ((n & 3) == 0) ST_SC1((v4u{d0, ta, d1, tb2}), WS_Q + (8 * X + (J - J_Q)) * 1024 + (g * 16 + 4 * ct + (n >> 2)) * 16);
    }
    FENCE();
    STAMP(4);
  }
  PACE_WAIT(); K_ISSUE(4); PACE_WAIT(); K_ISSUE(5);
  if (w == 0) {
    v4u qv[2];
    const bool need_ready = roleB || roleC;
    for (unsigned spin = 0; spin < SPIN_MAX; ++spin) {
      bool ok = true;
#pragma unroll
      for (int i = 0; i < 2; ++i) { const int gm = 64 * i + lane; qv[i] = LD_SC1(WS_Q + (8 * X + (gm >> 4)) * 1024 + ((J >> 3) * 16 + (gm & 15)) * 16); ok &= (qv[i].y == ta) & (qv[i].w == tb2); }
      if (need_ready) {
#pragma unroll
        for (int i = 0; i < 4; ++i) { const v4u rd = LD_SC1(WS_READY + (64 * i + lane) * 16); ok &= (rd.y == ta) & (rd.w == tb2); }
      }
      if (all_lanes(ok)) break;
      asm volatile("" ::: "memory");
    }
#pragma unroll
    for (int i = 0; i < 2; ++i) *(LAS v2u*)(L + L_Q + (64 * i + lane) * 8) = v2u{qv[i].x, qv[i].z};
    asm volatile("s_waitcnt lgkmcnt(0)" ::: "memory");
    if (lane == 0) *(volatile LAS unsigned*)(L + M_FLAG) = 1u;
    if (cu == 8 * J_B && lane == 0) __hip_atomic_store((GAS unsigned long long*)p.ws, E, __ATOMIC_RELAXED, __HIP_MEMORY_SCOPE_AGENT);
    FENCE();
    STAMP(5);
  }
  PACE_WAIT(); K_ISSUE(6); PACE_WAIT(); K_ISSUE(7);
  v4u vf[4][8];
#define V_ISSUE(u) do { unsigned vo_ = lane * 16; asm volatile("" : "+v"(vo_)); _Pragma("unroll") for (int j_ = 0; j_ < 8; ++j_) vf[u][j_] = __builtin_amdgcn_raw_buffer_load_b128(rv, vo_ + (j_ & 3) * 1024, kvbase + (2 * (u) + (j_ >> 2)) * 32768, AUX_NT); FENCE(); } while (0)
  PACE_WAIT(); V_ISSUE(0);
  STAMP(6);
  for (unsigned spin = 0; spin < (SPIN_MAX << 6); ++spin) {
    if (*(volatile LAS unsigned*)(L + M_FLAG) != 0u) break;
    __builtin_amdgcn_s_sleep(2);
  }
  asm volatile("" ::: "memory");
  STAMP(7);
  v4u gt[2] = {v4u{0u, 0u, 0u, 0u}, v4u{0u, 0u, 0u, 0u}};
  if (roleC) {
#pragma unroll
    for (int e = 0; e < 2; ++e) gt[e] = LD_SC1(WS_GATE + ((2 * w + e) * 64 + lane) * 16);
  }
  FENCE();
  const v4u qf0 = lds128(L + L_Q + (n & 3) * 256 + g * 32 + ((n >> 2) & 1) * 16), qf1 = lds128(L + L_Q + (n & 3) * 256 + 128 + g * 32 + ((n >> 2) & 1) * 16);
  float S[8][4];
#define QK(t) do { const f32x4 z_ = {0.f, 0.f, 0.f, 0.f}; \
    const f32x4 d0_ = mfma(kf[t][1], qf1, mfma(kf[t][0], qf0, z_)), d1_ = mfma(kf[t][3], qf1, mfma(kf[t][2], qf0, z_)); \
    _Pragma("unroll") for (int r_ = 0; r_ < 4; ++r_) { \
      auto s_ = __builtin_amdgcn_permlane32_swap(__float_as_uint(d0_[r_]), __float_as_uint(d1_[r_]), false, false); \
      S[t][r_] = __uint_as_float(s_[0]) + dpp<ROR4>(__uint_as_float(s_[1])); } } while (0)
  QK(0); QK(1); FENCE(); V_ISSUE(1);
  QK(2); QK(3); FENCE(); V_ISSUE(2);
  QK(4); QK(5); FENCE(); V_ISSUE(3);
  QK(6); QK(7); FENCE();
  STAMP(8);
  float mx = -__builtin_inff();
#pragma unroll
  for (int t = 0; t < 8; ++t)
#pragma unroll
    for (int r = 0; r < 4; r += 2) {
      const unsigned a = pk2(S[t][r], S[t][r + 1]);
      const unsigned b = pk2(bf_lo(a) * 0.08838834764831845f, bf_hi(a) * 0.08838834764831845f);
      S[t][r] = bf_lo(b); S[t][r + 1] = bf_hi(b);
      mx = fmaxf(mx, fmaxf(S[t][r], S[t][r + 1]));
    }
  mx = x32_max(x16_max(mx));
  float ls = 0.f;
  v4u pf[4];
#pragma unroll
  for (int u = 0; u < 4; ++u) {
    float e[8];
#pragma unroll
    for (int j = 0; j < 8; ++j) { e[j] = __builtin_amdgcn_exp2f((S[2 * u + (j >> 2)][j & 3] - mx) * LOG2E); ls += e[j]; }
    pf[u] = v4u{pk2(e[0], e[1]), pk2(e[2], e[3]), pk2(e[4], e[5]), pk2(e[6], e[7])};
  }
  ls = x32_sum(x16_sum(ls));
  if (lane < 4) *(LAS v2u*)(L + M_ML + w * 32 + lane * 8) = v2u{__float_as_uint(mx), __float_as_uint(ls)};
  if (roleC) {
    if (!all_lanes(umax4(gt[0]) != SENT && umax4(gt[1]) != SENT)) {
      for (unsigned spin = 0; spin < SPIN_MAX; ++spin) {
        asm volatile("" ::: "memory");
#pragma unroll
        for (int e = 0; e < 2; ++e) gt[e] = LD_SC1(WS_GATE + ((2 * w + e) * 64 + lane) * 16);
        if (all_lanes(umax4(gt[0]) != SENT && umax4(gt[1]) != SENT)) break;
      }
      asm volatile("s_waitcnt vmcnt(0)" ::: "memory");
    }
#pragma unroll
    for (int e = 0; e < 2; ++e) {
      const int gm = (2 * w + e) * 64 + lane;
#pragma unroll
      for (int b = 0; b < 4; ++b) *(LAS unsigned*)(L + L_GATE + b * 4096 + gm * 4) = gt[e][b];
    }
  }
  FENCE();
  STAMP(9);
  f32x4 oa[8];
#pragma unroll
  for (int d = 0; d < 8; ++d) oa[d] = f32x4{0.f, 0.f, 0.f, 0.f};
#pragma unroll
  for (int u = 0; u < 4; ++u) {
#pragma unroll
    for (int d = 0; d < 8; ++d) {
      const unsigned sel = (d & 1) ? 0x07060302u : 0x05040100u;
      v4u a;
#pragma unroll
      for (int i = 0; i < 4; ++i) a[i] = __builtin_amdgcn_perm(vf[u][2 * i + 1][d >> 1], vf[u][2 * i][d >> 1], sel);
      oa[d] = mfma(a, pf[u], oa[d]);
    }
    FENCE();
  }
  STAMP(10);
  int tid2 = threadIdx.x; asm volatile("" : "+v"(tid2));
#define tid tid2
  const int lane2 = tid2 & 63;
#define lane lane2
  const int g2 = lane2 >> 4, n2 = lane2 & 15;
#define g g2
#define n n2
  const int tb = tid2 >> 7, tk = 4 * (tid2 & 127);
  if (n < 4) {
#pragma unroll
    for (int r = 0; r < 4; ++r) {
      *(LAS f32x4*)(L + L_MRG + w * MRG_W + n * MRG_H + r * 128 + g * 32) = f32x4{oa[0][r], oa[1][r], oa[2][r], oa[3][r]};
      *(LAS f32x4*)(L + L_MRG + w * MRG_W + n * MRG_H + r * 128 + g * 32 + 16) = f32x4{oa[4][r], oa[5][r], oa[6][r], oa[7][r]};
    }
  }
  bar();
  if (w < 2) {
    const int h = tid >> 5, pos = 4 * (tid & 31);
    float mw[8], M = -__builtin_inff();
#pragma unroll
    for (int ww = 0; ww < 8; ++ww) { mw[ww] = *(LAS float*)(L + M_ML + ww * 32 + h * 8); M = fmaxf(M, mw[ww]); }
    f32x4 o = {0.f, 0.f, 0.f, 0.f}; float Ls = 0.f;
#pragma unroll
    for (int ww = 0; ww < 8; ++ww) {
      const float f = __builtin_amdgcn_exp2f((mw[ww] - M) * LOG2E);
      Ls += f * *(LAS float*)(L + M_ML + ww * 32 + h * 8 + 4);
      o += f * *(LAS f32x4*)(L + L_MRG + ww * MRG_W + h * MRG_H + pos * 4);
    }
    ST_SC1((v4u{nosent(o.x), nosent(o.y), nosent(o.z), nosent(o.w)}), WS_PART + cu * 2048 + h * 512 + pos * 4);
    if ((tid & 31) == 0) __builtin_amdgcn_raw_buffer_store_b64((v2u{nosent(M), nosent(Ls)}), rws, WS_STAT + cu * 32 + h * 8, 0, AUX_SC1);
  }
  STAMP(11);
  if (!(roleB || roleC)) {
    return;
  }
  asm volatile("s_barrier" ::: "memory");
  if (roleB) {
    {
      const int r0 = 64 * w + (lane >> 3);
      const GAS unsigned char* s = (const GAS unsigned char*)p.wo + ((size_t)(512 * X + r0) * 512 + 64 * (J - J_B) + 8 * img_chunk(lane, r0)) * 2;
#pragma unroll
      for (int e = 0; e < 8; ++e) glds16((const void*)(s + (size_t)e * 8 * 512 * 2), lds0 + L_W + w * 8192 + e * 1024);
    }
    const int bb = w >> 1, hf = lane >> 5;
    v4u po[2][4]; v2u st[2];
    for (unsigned spin = 0; spin < SPIN_MAX; ++spin) {
      unsigned m = 0;
#pragma unroll
      for (int hh = 0; hh < 2; ++hh) {
        const int lh = 2 * (w & 1) + hh;
#pragma unroll
        for (int i = 0; i < 4; ++i) { po[hh][i] = LD_SC1(WS_PART + ((8 * bb + 2 * i + hf) * 8 + X) * 2048 + lh * 512 + (lane & 31) * 16); m = max(m, umax4(po[hh][i])); }
        st[hh] = __builtin_amdgcn_raw_buffer_load_b64(rws, WS_STAT + ((8 * bb + 2 * (lane & 3) + hf) * 8 + X) * 32 + lh * 8, 0, AUX_SC1);
        m = max(m, max(st[hh].x, st[hh].y));
      }
      if (all_lanes(m != SENT)) break;
      asm volatile("" ::: "memory");
    }
    STAMP(12);
    asm volatile("s_waitcnt vmcnt(0)" ::: "memory");
    v4u bo[2][4];
#pragma unroll
    for (int ct = 0; ct < 4; ++ct) { lptr wp = L + L_W + w * 8192 + frag_base(lane, ct); bo[0][ct] = w_frag(wp, 0); bo[1][ct] = w_frag(wp, 1); }
    STAMP(19);
#pragma unroll
    for (int hh = 0; hh < 2; ++hh) {
      const float ms = __uint_as_float(st[hh].x);
      float M = fmaxf(ms, dpp<XOR1>(ms)); M = fmaxf(M, dpp<XOR2>(M)); M = x32_max(M);
      const float f = __builtin_amdgcn_exp2f((ms - M) * LOG2E);
      float Ls = f * __uint_as_float(st[hh].y); Ls += dpp<XOR1>(Ls); Ls += dpp<XOR2>(Ls); Ls = x32_sum(Ls);
      const float wt = f / Ls;
      const float w0 = dpp<0x00>(wt), w1 = dpp<0x55>(wt), w2 = dpp<0xAA>(wt), w3 = dpp<0xFF>(wt);
      float o[4];
#pragma unroll
      for (int e = 0; e < 4; ++e)
        o[e] = x32_sum(w0 * __uint_as_float(po[hh][0][e]) + w1 * __uint_as_float(po[hh][1][e]) + w2 * __uint_as_float(po[hh][2][e]) + w3 * __uint_as_float(po[hh][3][e]));
      const int pi = 4 * (lane & 31), dim = 32 * ((pi >> 3) & 3) + 8 * (pi >> 5) + (pi & 7);
      if (lane < 32) *(LAS v2u*)(L + L_ACT + bb * RS + act_off(128 * (2 * (w & 1) + hh) + dim)) = v2u{pk2(o[0], o[1]), pk2(o[2], o[3])};
    }
    STAMP(20);
    bar();
    STAMP(21);
    {
      lptr ap = L + L_ACT + (lane & 3) * RS + g * 16 + (2 * w) * 64;
      const v4u a0 = lds128(ap), a1 = lds128(ap + 64);
#pragma unroll
      for (int ct = 0; ct < 4; ++ct) {
        f32x4 d = mfma(a0, bo[0][ct], f32x4{0.f, 0.f, 0.f, 0.f});
        d = mfma(a1, bo[1][ct], d);
        *(LAS float*)(L + L_RED + w * 1024 + g * 256 + (16 * ct + n) * 4) = sel4(d, g);
      }
    }
    bar();
    STAMP(22);
    if (tid < 64) {
      f32x4 s = {0.f, 0.f, 0.f, 0.f};
#pragma unroll
      for (int ww = 0; ww < 8; ++ww) s += *(LAS f32x4*)(L + L_RED + ww * 1024 + tid * 16);
      ST_SC1((v4u{nosent(s.x), nosent(s.y), nosent(s.z), nosent(s.w)}), WS_ATT + ((X * 4 + g) * 512 + 64 * (J - J_B) + 4 * n) * 4);
    }
    STAMP(13);
    return;
  }
  const int pc = 7 * X + (J - J_C);
  {
    const int r0 = 64 * w + (lane >> 3), ch = img_chunk(lane, r0);
    const GAS unsigned char* s = (const GAS unsigned char*)((ch >> 2) ? p.wu : p.wg) + ((size_t)r0 * 1792 + 32 * pc + 8 * (ch & 3)) * 2;
#pragma unroll
    for (int e = 0; e < 8; ++e) glds16((const void*)(s + (size_t)e * 8 * 1792 * 2), lds0 + L_W + w * 8192 + e * 1024);
    const int rd = lane >> 3;
    const GAS unsigned char* sd = (const GAS unsigned char*)p.wd + ((size_t)(32 * pc + rd) * 512 + 64 * w + 8 * img_chunk(lane, rd)) * 2;
#pragma unroll
    for (int e = 0; e < 4; ++e) glds16((const void*)(sd + (size_t)e * 8 * 512 * 2), lds0 + L_WD + w * 4096 + e * 1024);
  }
  const v2u xr = *(const GAS v2u*)((const GAS unsigned char*)p.x + (tb * 512 + tk) * 2);
  STAMP(12); STAMP(13);
  {
    v4u at[8];
    for (unsigned spin = 0; spin < SPIN_MAX; ++spin) {
      unsigned m = 0;
#pragma unroll
      for (int i = 0; i < 8; ++i) { at[i] = LD_SC1(WS_ATT + ((i * 4 + tb) * 512 + tk) * 4); m = max(m, umax4(at[i])); }
      if (all_lanes(m != SENT)) break;
      asm volatile("" ::: "memory");
    }
    STAMP(14);
    asm volatile("s_waitcnt vmcnt(0)" ::: "memory");
    v4u bg[2][4], bd[4];
#pragma unroll
    for (int ct = 0; ct < 4; ++ct) { lptr wp = L + L_W + w * 8192 + frag_base(lane, ct); bg[0][ct] = w_frag(wp, 0); bg[1][ct] = w_frag(wp, 1); }
    {
      const int q = (lane >> 2) & 3;
#pragma unroll
      for (int kp = 0; kp < 4; ++kp) {
        const int ra = 8 * kp + q, rb_ = ra + 4;
        bd[kp] = tr_frag(L + L_WD + w * 4096 + ra * 128 + ((g ^ ((ra >> 1) & 3)) * 32) + (lane & 3) * 8, L + L_WD + w * 4096 + rb_ * 128 + ((g ^ ((rb_ >> 1) & 3)) * 32) + (lane & 3) * 8);
      }
    }
    STAMP(19);
    const v2u ga = *(LAS v2u*)(L + L_GATE + tb * 4096 + tk * 2), sb = *(LAS v2u*)(L + L_GATE + tb * 4096 + 1024 + tk * 2), cb = *(LAS v2u*)(L + L_GATE + tb * 4096 + 2048 + tk * 2);
    const float gav[4] = {bf_lo(ga.x), bf_hi(ga.x), bf_lo(ga.y), bf_hi(ga.y)}, sbv[4] = {bf_lo(sb.x), bf_hi(sb.x), bf_lo(sb.y), bf_hi(sb.y)},
                cbv[4] = {bf_lo(cb.x), bf_hi(cb.x), bf_lo(cb.y), bf_hi(cb.y)}, xs[4] = {bf_lo(xr.x), bf_hi(xr.x), bf_lo(xr.y), bf_hi(xr.y)};
    float x2[4];
#pragma unroll
    for (int e = 0; e < 4; ++e) {
      float a = 0.f;
#pragma unroll
      for (int i = 0; i < 8; ++i) a += __uint_as_float(at[i][e]);
      x2[e] = rb(xs[e] + rb(gav[e] * rb(a)));
    }
    *(LAS v2u*)(L + L_X + (tb * 512 + tk) * 2) = v2u{pk2(x2[0], x2[1]), pk2(x2[2], x2[3])};
    float sum, m2;
    ln_wave_stats(x2, sum, m2);
    if (lane == 0) *(LAS v2u*)(L + M_LN + w * 8) = v2u{__float_as_uint(sum), __float_as_uint(m2)};
    bar();
    STAMP(20);
    float mean_r, den_r;
    ln_row(L, tb, mean_r, den_r);
    float h2[4];
#pragma unroll
    for (int e = 0; e < 4; ++e) h2[e] = ln_mod(x2[e], mean_r, den_r, cbv[e], sbv[e]);
    *(LAS v2u*)(L + L_ACT + tb * RS + act_off(tk)) = v2u{pk2(h2[0], h2[1]), pk2(h2[2], h2[3])};
    bar();
    STAMP(21);
    {
      lptr ap = L + L_ACT + (lane & 3) * RS + g * 16 + (2 * w) * 64;
      const v4u a0 = lds128(ap), a1 = lds128(ap + 64);
#pragma unroll
      for (int ct = 0; ct < 4; ++ct) {
        f32x4 d = mfma(a0, bg[0][ct], f32x4{0.f, 0.f, 0.f, 0.f});
        d = mfma(a1, bg[1][ct], d);
        *(LAS float*)(L + L_RED + w * 1024 + g * 256 + (16 * ct + n) * 4) = sel4(d, g);
      }
    }
    bar();
    STAMP(22);
    if (w < 2) {
      const int b = 2 * w + (lane >> 5), un = lane & 31;
      float s = 0.f, up = 0.f;
#pragma unroll
      for (int ww = 0; ww < 8; ++ww) { s += *(LAS float*)(L + L_RED + ww * 1024 + b * 256 + un * 4); up += *(LAS float*)(L + L_RED + ww * 1024 + b * 256 + 128 + un * 4); }
      s = rb(s); up = rb(up);
      const float u = 0.7978845608028654f * (s + 0.044715f * s * s * s);
      const float ge = rb(s / (1.f + __builtin_amdgcn_exp2f(-2.f * LOG2E * u)));
      *(LAS unsigned short*)(L + M_HID + b * 64 + un * 2) = (unsigned short)(__float_as_uint(rb(ge * up)) >> 16);
    }
    bar();
    STAMP(23);
    f32x4 d = {0.f, 0.f, 0.f, 0.f};
#pragma unroll
    for (int kp = 0; kp < 4; ++kp) {
      v4u a4 = lds128(L + M_HID + (n & 3) * 64 + kp * 16);
      if ((n >> 2) != g) a4 = v4u{0u, 0u, 0u, 0u};
      d = mfma(a4, bd[kp], d);
    }
    ST_SC1((v4u{nosent(d[0]), nosent(d[1]), nosent(d[2]), nosent(d[3])}), WS_DOWN + pc * 8192 + (64 * w + lane) * 16);
  }
  STAMP(15);
  if (J < J_F_END) {
    const int f = 4 * X + (J - J_C);
    if (w < 7) {
      v4u dv[2];
      for (unsigned spin = 0; spin < SPIN_MAX; ++spin) {
#pragma unroll
        for (int i = 0; i < 2; ++i) dv[i] = LD_SC1(WS_DOWN + (28 * i + 4 * w + g) * 8192 + (16 * f + n) * 16);
        if (all_lanes(max(umax4(dv[0]), umax4(dv[1])) != SENT)) break;
        asm volatile("" ::: "memory");
      }
      f32x4 s;
#pragma unroll
      for (int b = 0; b < 4; ++b) s[b] = x32_sum(x16_sum(__uint_as_float(dv[0][b]) + __uint_as_float(dv[1][b])));
      if (lane < 16) *(LAS f32x4*)(L + L_RED + w * 256 + lane * 16) = s;
    }
    bar();
    if (tid < 64) {
      const int b = tid >> 4, col = 16 * f + n;
      float s = 0.f;
#pragma unroll
      for (int ww = 0; ww < 7; ++ww) s += *(LAS float*)(L + L_RED + ww * 256 + n * 16 + b * 4);
      const float y = rb(s);
      const float gb = bf_lo(*(LAS unsigned short*)(L + L_GATE + b * 4096 + 3072 + col * 2));
      const float x2 = bf_lo(*(LAS unsigned short*)(L + L_X + (b * 512 + col) * 2));
      const unsigned ob = pk2(x2 + rb(gb * y), 0.f) & 0xffffu;
      const unsigned o2 = ob | (dppu<XOR1>(ob) << 16);
      if ((n & 1) == 0) *(GAS unsigned*)((GAS unsigned char*)p.out + (b * 512 + col) * 2) = o2;
    }
  }
#undef tid
#undef lane
#undef g
#undef n
}
}

extern "C" void kernel_launch(void* const* d_in, const int* in_sizes, int n_in, void* d_out, int out_size, void* d_ws, size_t ws_size, hipStream_t stream) {
  (void)in_sizes; (void)n_in; (void)out_size; (void)ws_size;
  Params p;
  p.x = d_in[0]; p.k = d_in[1]; p.v = d_in[2]; p.wq = d_in[3]; p.wo = d_in[4];
  p.wg = d_in[6]; p.wu = d_in[7]; p.wd = d_in[8]; p.c = d_in[9]; p.wm = d_in[10];
  p.out = d_out; p.ws = d_ws;
  hipLaunchKernelGGL(adaln_block_kernel, dim3(NCU), dim3(NTHR), 0, stream, p);
}
```

```cpp
#include <hip/hip_runtime.h>
#include <hip/hip_bf16.h>
#include <stdint.h>

#define GAS __attribute__((address_space(1)))
#define LAS __attribute__((address_space(3)))
typedef unsigned v4u __attribute__((ext_vector_type(4)));
typedef unsigned v2u __attribute__((ext_vector_type(2)));
typedef float f32x4 __attribute__((ext_vector_type(4)));
typedef short s16x4 __attribute__((ext_vector_type(4)));
typedef __bf16 bf16x8 __attribute__((ext_vector_type(8)));
typedef __bf16 bf16x2 __attribute__((ext_vector_type(2)));
typedef LAS unsigned char* lptr;

#define PACE 4
#define STAMPS 0

namespace {
constexpr int NCU = 256, NTHR = 512;
constexpr unsigned KV_BYTES = 4u * 8u * 8192u * 128u * 2u;
constexpr unsigned WS_EPOCH = 0, WS_READY = 4096, WS_H = 8192, WS_Q = 16384, WS_GATE = WS_Q + 65536, WS_STAT = WS_GATE + 16384, WS_ATT = WS_STAT + 8192,
                   WS_PART = WS_ATT + 65536, WS_DOWN = WS_PART + 256 * 2048, WS_END = WS_DOWN + 56 * 8192, WS_STAMP = WS_END + 65536;
constexpr int RS = 1088;
constexpr int L_W = 0, L_WD = 65536, L_MRG = 98304, L_GATE = L_MRG + 17408, L_ACT = L_GATE + 16384, L_X = L_ACT + 4352, L_Q = L_X + 4096, L_RED = L_Q + 1024,
              L_MISC = L_RED + 8192, L_BYTES = L_MISC + 1024;
constexpr int MRG_H = 528, MRG_W = 4 * MRG_H;
constexpr int M_LN = L_MISC, M_ML = L_MISC + 64, M_HID = L_MISC + 320, M_FLAG = L_MISC + 576;
constexpr unsigned SENT = 0xFFFFFFFFu;
constexpr unsigned SPIN_MAX = 1u << 15;
constexpr int AUX_NT = 2, AUX_SC1 = 16;
constexpr float LOG2E = 1.4426950408889634f;
constexpr int J_H = 0, J_G = 2, J_Q = 6, J_B = 14, J_C = 22, J_F_END = 26, J_C_END = 29;

struct Params { const void* x; const void* k; const void* v; const void* wq; const void* wo; const void* wg; const void* wu; const void* wd; const void* c; const void* wm; void* out; void* ws; };

__device__ __forceinline__ float bf_lo(unsigned u) { return __builtin_bit_cast(float, u << 16); }
__device__ __forceinline__ float bf_hi(unsigned u) { return __builtin_bit_cast(float, u & 0xffff0000u); }
__device__ __forceinline__ unsigned pk2(float a, float b) { bf16x2 r = {(__bf16)a, (__bf16)b}; return __builtin_bit_cast(unsigned, r); }
__device__ __forceinline__ float rb(float a) { return bf_lo(pk2(a, a)); }
template <int CTRL> __device__ __forceinline__ float dpp(float x) { return __builtin_bit_cast(float, __builtin_amdgcn_mov_dpp(__builtin_bit_cast(int, x), CTRL, 0xf, 0xf, true)); }
template <int CTRL> __device__ __forceinline__ unsigned dppu(unsigned x) { return (unsigned)__builtin_amdgcn_mov_dpp((int)x, CTRL, 0xf, 0xf, true); }
constexpr int XOR1 = 0xB1, XOR2 = 0x4E, HMIR = 0x141, ROR4 = 0x124, ROR8 = 0x128;
__device__ __forceinline__ float x16_sum(float x) { auto s = __builtin_amdgcn_permlane16_swap(__float_as_uint(x), __float_as_uint(x), false, false); return __uint_as_float(s[0]) + __uint_as_float(s[1]); }
__device__ __forceinline__ float x32_sum(float x) { auto s = __builtin_amdgcn_permlane32_swap(__float_as_uint(x), __float_as_uint(x), false, false); return __uint_as_float(s[0]) + __uint_as_float(s[1]); }
__device__ __forceinline__ float x16_max(float x) { auto s = __builtin_amdgcn_permlane16_swap(__float_as_uint(x), __float_as_uint(x), false, false); return fmaxf(__uint_as_float(s[0]), __uint_as_float(s[1])); }
__device__ __forceinline__ float x32_max(float x) { auto s = __builtin_amdgcn_permlane32_swap(__float_as_uint(x), __float_as_uint(x), false, false); return fmaxf(__uint_as_float(s[0]), __uint_as_float(s[1])); }
__device__ __forceinline__ float wave_sum(float x) { x += dpp<XOR1>(x); x += dpp<XOR2>(x); x += dpp<HMIR>(x); x += dpp<ROR8>(x); return x32_sum(x16_sum(x)); }
__device__ __forceinline__ f32x4 mfma(v4u a, v4u b, f32x4 c) { return __builtin_amdgcn_mfma_f32_16x16x32_bf16(__builtin_bit_cast(bf16x8, a), __builtin_bit_cast(bf16x8, b), c, 0, 0, 0); }
__device__ __forceinline__ v2u tr_read(lptr p) { s16x4 r = __builtin_amdgcn_ds_read_tr16_b64_v4i16((LAS s16x4*)p); return __builtin_bit_cast(v2u, r); }
__device__ __forceinline__ v4u tr_frag(lptr p0, lptr p1) { v2u a = tr_read(p0), b = tr_read(p1); return v4u{a.x, a.y, b.x, b.y}; }
__device__ __forceinline__ v4u lds128(lptr p) { return *(LAS v4u*)p; }
__device__ __forceinline__ void bar() { asm volatile("s_waitcnt lgkmcnt(0)\n\ts_barrier" ::: "memory"); }
__device__ __forceinline__ void glds16(const void* gsrc, unsigned lds_dst) {
  unsigned keep;
  asm volatile("s_mov_b32 %0, m0\n\ts_mov_b32 m0, %2\n\ts_nop 0\n\tglobal_load_lds_dwordx4 %1, off\n\ts_mov_b32 m0, %0" : "=&s"(keep) : "v"(gsrc), "s"(lds_dst) : "memory");
}
__device__ __forceinline__ unsigned nosent(float f) { unsigned u = __float_as_uint(f); return u == SENT ? SENT - 1u : u; }
__device__ __forceinline__ unsigned nosentu(unsigned u) { return u == SENT ? SENT - 1u : u; }
__device__ __forceinline__ unsigned umax4(v4u a) { return max(max(a.x, a.y), max(a.z, a.w)); }
__device__ __forceinline__ bool all_lanes(bool ok) { return __builtin_amdgcn_ballot_w64(!ok) == 0ull; }
__device__ __forceinline__ float sel4(f32x4 d, int g) { return g == 0 ? d[0] : g == 1 ? d[1] : g == 2 ? d[2] : d[3]; }
#define RSRC(p, n) __builtin_amdgcn_make_buffer_rsrc((void*)(p), 0, (int)(n), 0x00020000)
#define LD_SC1(off) __builtin_amdgcn_raw_buffer_load_b128(rws, (off), 0, AUX_SC1)
#define ST_SC1(v, off) __builtin_amdgcn_raw_buffer_store_b128((v), rws, (off), 0, AUX_SC1)
#define FENCE() __builtin_amdgcn_sched_barrier(0)
#define STAMP(i) do { } while (0)

__device__ __forceinline__ int img_chunk(int lane, int row) { return ((((lane & 7) >> 1) ^ ((row >> 1) & 3)) << 1) | (lane & 1); }
__device__ __forceinline__ int frag_base(int lane, int ct) { const int g = lane >> 4, q = (lane >> 2) & 3; return (4 * g + q) * 128 + ((ct ^ ((2 * g + (q >> 1)) & 3)) * 32) + (lane & 3) * 8; }
__device__ __forceinline__ v4u w_frag(lptr base, int s) { return tr_frag(base + s * 4096, base + s * 4096 + 2048); }
__device__ __forceinline__ int act_off(int k) { return (k >> 5) * 64 + ((k >> 2) & 3) * 16 + ((k >> 4) & 1) * 8; }
__device__ __forceinline__ f32x4 gemv_tile(lptr W, lptr act, int lane, int ct) {
  lptr ap = act + (lane & 3) * RS + (lane >> 4) * 16;
  lptr wp = W + frag_base(lane, ct);
  f32x4 a0 = {0.f, 0.f, 0.f, 0.f}, a1 = {0.f, 0.f, 0.f, 0.f};
#pragma unroll
  for (int s = 0; s < 16; s += 2) {
    a0 = mfma(lds128(ap + s * 64), w_frag(wp, s), a0);
    a1 = mfma(lds128(ap + s * 64 + 64), w_frag(wp, s + 1), a1);
  }
  return a0 + a1;
}
__device__ __forceinline__ void ln_wave_stats(const float (&v)[4], float& sum, float& m2) {
  sum = wave_sum((v[0] + v[1]) + (v[2] + v[3]));
  const float mw = sum * (1.f / 256.f);
  const float d0 = v[0] - mw, d1 = v[1] - mw, d2 = v[2] - mw, d3 = v[3] - mw;
  m2 = wave_sum((d0 * d0 + d1 * d1) + (d2 * d2 + d3 * d3));
}
__device__ __forceinline__ void ln_row(lptr L, int b, float& mean_r, float& den_r) {
  const f32x4 s = *(LAS f32x4*)(L + M_LN + b * 16);
  const float dm = (s.x - s.z) * (1.f / 256.f);
  const float mean = (s.x + s.z) * (1.f / 512.f);
  const float var = (s.y + s.w + dm * dm * 128.f) * (1.f / 512.f);
  mean_r = rb(mean);
  den_r = rb(__builtin_sqrtf(rb(rb(var) + 1e-5f)));
}
__device__ __forceinline__ float ln_mod(float x, float mean_r, float den_r, float cc, float ss) {
  const float ln = rb(rb(x - mean_r) / den_r);
  return rb(rb(ln * rb(1.f + cc)) + ss);
}

__global__ void __launch_bounds__(NTHR, 2) adaln_block_kernel(Params p) {
  __shared__ __attribute__((aligned(16))) unsigned char smem[L_BYTES + (STAMPS ? 1024 : 0)];
  lptr L = (lptr)smem;
  const unsigned lds0 = (unsigned)(uintptr_t)L;
  const int tid = threadIdx.x, lane = tid & 63, w = __builtin_amdgcn_readfirstlane(tid >> 6);
  const int cu = blockIdx.x, X = cu & 7, J = cu >> 3;
  const int g = lane >> 4, n = lane & 15;
  const auto rws = RSRC(p.ws, WS_END);
  const auto rk = RSRC(p.k, KV_BYTES);
  const auto rv = RSRC(p.v, KV_BYTES);
  const bool roleH = J < J_G, roleG = J >= J_G && J < J_Q, roleM = J < J_Q, roleQ = J >= J_Q && J < J_B, roleB = J >= J_B && J < J_C, roleC = J >= J_C && J < J_C_END;
  STAMP(0);
  const unsigned long long ep = __hip_atomic_load((GAS unsigned long long*)p.ws, __ATOMIC_RELAXED, __HIP_MEMORY_SCOPE_AGENT);
  v2u cv = {0u, 0u}, xv = {0u, 0u};
  if (roleM) {
    cv = *(const GAS v2u*)((const GAS unsigned char*)p.c + ((tid >> 7) * 512 + 4 * (tid & 127)) * 2);
    if (roleH) xv = *(const GAS v2u*)((const GAS unsigned char*)p.x + ((tid >> 7) * 512 + 4 * (tid & 127)) * 2);
  }
  if (roleM || roleQ) {
    const int r0 = 64 * w + (lane >> 3), ch = img_chunk(lane, r0);
    const GAS unsigned char* s;
    size_t rstride;
    if (roleH) { s = (const GAS unsigned char*)p.wm + ((size_t)r0 * 3072 + (ch >> 2) * 512 + 32 * (2 * X + J) + 8 * (ch & 3)) * 2; rstride = 3072 * 2; }
    else if (roleG) { s = (const GAS unsigned char*)p.wm + ((size_t)r0 * 3072 + 1024 + 64 * (4 * X + (J - J_G)) + 8 * ch) * 2; rstride = 3072 * 2; }
    else { s = (const GAS unsigned char*)p.wq + ((size_t)r0 * 4096 + 64 * (8 * X + (J - J_Q)) + 8 * ch) * 2; rstride = 4096 * 2; }
#pragma unroll
    for (int e = 0; e < 8; ++e) glds16((const void*)(s + (size_t)e * 8 * rstride), lds0 + L_W + w * 8192 + e * 1024);
    asm volatile("s_barrier" ::: "memory");
  }
  if (w == 1) {
    const v4u sent = {SENT, SENT, SENT, SENT};
    ST_SC1(sent, WS_PART + cu * 2048 + lane * 16);
    ST_SC1(sent, WS_PART + cu * 2048 + 1024 + lane * 16);
    if (lane < 2) ST_SC1(sent, WS_STAT + cu * 32 + lane * 16);
    if (roleB) ST_SC1(sent, WS_ATT + ((X * 4 + g) * 512 + 64 * (J - J_B) + 4 * n) * 4);
    if (roleG && lane < 32) ST_SC1(sent, WS_GATE + (4 * X + (J - J_G)) * 512 + lane * 16);
    if (roleC) {
#pragma unroll
      for (int e = 0; e < 8; ++e) ST_SC1(sent, WS_DOWN + (7 * X + (J - J_C)) * 8192 + e * 1024 + lane * 16);
    }
  }
  FENCE();
  const unsigned kvbase = (((unsigned)(J >> 3) * 8u + (unsigned)X) * 8192u + (unsigned)(J & 7) * 1024u) * 256u + (unsigned)w * 4096u;
  const unsigned koff = (4 * (lane & 3) + ((lane >> 2) & 1)) * 256 + (lane >> 3) * 16;
  v4u kf[8][4];
#define K_ISSUE(t) do { unsigned ko_ = koff; asm volatile("" : "+v"(ko_)); _Pragma("unroll") for (int q_ = 0; q_ < 4; ++q_) kf[t][q_] = __builtin_amdgcn_raw_buffer_load_b128(rk, ko_ + (q_ >> 1) * 512 + (q_ & 1) * 128, kvbase + (t) * 32768, AUX_NT); FENCE(); } while (0)
  K_ISSUE(0); K_ISSUE(1);
  if (tid == 0) *(LAS unsigned*)(L + M_FLAG) = 0u;
  const unsigned long long E = ep + 1ull;
  const unsigned ta = __builtin_amdgcn_readfirstlane((unsigned)E), tb2 = __builtin_amdgcn_readfirstlane(((unsigned)E * 0x9E3779B1u) ^ (unsigned)(E >> 32));
  if (roleM) {
    const int tb = tid >> 7, tk = 4 * (tid & 127);
    float cs[4] = {bf_lo(cv.x), bf_hi(cv.x), bf_lo(cv.y), bf_hi(cv.y)};
#pragma unroll
    for (int e = 0; e < 4; ++e) cs[e] = cs[e] / (1.f + __expf(-cs[e]));
    *(LAS v2u*)(L + L_ACT + tb * RS + act_off(tk)) = v2u{pk2(cs[0], cs[1]), pk2(cs[2], cs[3])};
    if (roleH) {
      *(LAS v2u*)(L + L_X + (tb * 512 + tk) * 2) = xv;
      const float xs[4] = {bf_lo(xv.x), bf_hi(xv.x), bf_lo(xv.y), bf_hi(xv.y)};
      float sum, m2;
      ln_wave_stats(xs, sum, m2);
      if (lane == 0) *(LAS v2u*)(L + M_LN + w * 8) = v2u{__float_as_uint(sum), __float_as_uint(m2)};
    }
  }
  STAMP(17);
  if (roleM || roleQ) asm volatile("s_waitcnt vmcnt(8)" ::: "memory");
  STAMP(18);
  bar();
  STAMP(1);
  if (roleH && w < 2) {
    const float ss = rb(sel4(gemv_tile(L + L_W, L + L_ACT, lane, w), g));
    const float cc = rb(sel4(gemv_tile(L + L_W, L + L_ACT, lane, w + 2), g));
    float mean_r, den_r;
    ln_row(L, g, mean_r, den_r);
    const int col = 32 * (2 * X + J) + 16 * w + n;
    const float xx = bf_lo(*(LAS unsigned short*)(L + L_X + (g * 512 + col) * 2));
    const unsigned hb = __float_as_uint(ln_mod(xx, mean_r, den_r, cc, ss)) >> 16;
    const unsigned d0 = dppu<0x00>(hb) | (dppu<0x55>(hb) << 16), d1 = dppu<0xAA>(hb) | (dppu<0xFF>(hb) << 16);
    if ((n & 3) == 0) ST_SC1((v4u{d0, ta, d1, tb2}), WS_H + (col >> 3) * 128 + (g * 2 + ((n >> 2) & 1)) * 16);
    FENCE();
  }
  if (roleG && w < 4) {
    const f32x4 d = gemv_tile(L + L_W, L + L_ACT, lane, w);
    unsigned o[4];
#pragma unroll
    for (int b = 0; b < 4; ++b) { const unsigned hb = __float_as_uint(rb(d[b])) >> 16; o[b] = nosentu(hb | (dppu<XOR1>(hb) << 16)); }
    if (lane < 16 && (lane & 1) == 0) *(LAS v4u*)(L + L_RED + (8 * w + (lane >> 1)) * 16) = v4u{o[0], o[1], o[2], o[3]};
    FENCE();
  }
  if (roleG) bar();
  STAMP(2);
#define PACE_WAIT() do { if (PACE >= 0) asm volatile("s_waitcnt vmcnt(%0)" ::"i"(PACE < 0 ? 0 : PACE) : "memory"); } while (0)
  if (w == 1) {
    asm volatile("s_waitcnt vmcnt(8)" ::: "memory");
    if (roleG && lane < 32) ST_SC1(lds128(L + L_RED + lane * 16), WS_GATE + (4 * X + (J - J_G)) * 512 + lane * 16);
    if (lane == 0) ST_SC1((v4u{0u, ta, 0u, tb2}), WS_READY + cu * 16);
    FENCE();
  }
  PACE_WAIT(); K_ISSUE(2); PACE_WAIT(); K_ISSUE(3);
  if (w == 0 && roleQ) {
    v4u hv[8];
    for (unsigned spin = 0; spin < SPIN_MAX; ++spin) {
      bool ok = true;
#pragma unroll
      for (int i = 0; i < 8; ++i) { hv[i] = LD_SC1(WS_H + (64 * i + lane) * 16); ok &= (hv[i].y == ta) & (hv[i].w == tb2); }
      if (all_lanes(ok)) break;
      asm volatile("" ::: "memory");
    }
    STAMP(3);
#pragma unroll
    for (int i = 0; i < 8; ++i) { const int gm = 64 * i + lane; *(LAS v2u*)(L + L_ACT + ((gm >> 1) & 3) * RS + act_off(8 * (gm >> 3) + 4 * (gm & 1))) = v2u{hv[i].x, hv[i].z}; }
    asm volatile("s_waitcnt lgkmcnt(0)" ::: "memory");
#pragma unroll
    for (int ct = 0; ct < 4; ++ct) {
      const f32x4 d = gemv_tile(L + L_W, L + L_ACT, lane, ct);
      const unsigned qb = __float_as_uint(rb(sel4(d, g))) >> 16;
      const unsigned d0 = dppu<0x00>(qb) | (dppu<0x55>(qb) << 16), d1 = dppu<0xAA>(qb) | (dppu<0xFF>(qb) << 16);
      if ((n & 3) == 0) ST_SC1((v4u{d0, ta, d1, tb2}), WS_Q + (8 * X + (J - J_Q)) * 1024 + (g * 16 + 4 * ct + (n >> 2)) * 16);
    }
    FENCE();
    STAMP(4);
  }
  PACE_WAIT(); K_ISSUE(4); PACE_WAIT(); K_ISSUE(5);
  if (w == 0) {
    v4u qv[2];
    const bool need_ready = roleB || roleC;
    for (unsigned spin = 0; spin < SPIN_MAX; ++spin) {
      bool ok = true;
#pragma unroll
      for (int i = 0; i < 2; ++i) { const int gm = 64 * i + lane; qv[i] = LD_SC1(WS_Q + (8 * X + (gm >> 4)) * 1024 + ((J >> 3) * 16 + (gm & 15)) * 16); ok &= (qv[i].y == ta) & (qv[i].w == tb2); }
      if (need_ready) {
#pragma unroll
        for (int i = 0; i < 4; ++i) { const v4u rd = LD_SC1(WS_READY + (64 * i + lane) * 16); ok &= (rd.y == ta) & (rd.w == tb2); }
      }
      if (all_lanes(ok)) break;
      asm volatile("" ::: "memory");
    }
#pragma unroll
    for (int i = 0; i < 2; ++i) *(LAS v2u*)(L + L_Q + (64 * i + lane) * 8) = v2u{qv[i].x, qv[i].z};
    asm volatile("s_waitcnt lgkmcnt(0)" ::: "memory");
    if (lane == 0) *(volatile LAS unsigned*)(L + M_FLAG) = 1u;
    if (cu == 8 * J_B && lane == 0) __hip_atomic_store((GAS unsigned long long*)p.ws, E, __ATOMIC_RELAXED, __HIP_MEMORY_SCOPE_AGENT);
    FENCE();
    STAMP(5);
  }
  PACE_WAIT(); K_ISSUE(6); PACE_WAIT(); K_ISSUE(7);
  v4u vf[4][8];
#define V_ISSUE(u) do { unsigned vo_ = lane * 16; asm volatile("" : "+v"(vo_)); _Pragma("unroll") for (int j_ = 0; j_ < 8; ++j_) vf[u][j_] = __builtin_amdgcn_raw_buffer_load_b128(rv, vo_ + (j_ & 3) * 1024, kvbase + (2 * (u) + (j_ >> 2)) * 32768, AUX_NT); FENCE(); } while (0)
  PACE_WAIT(); V_ISSUE(0);
  STAMP(6);
  for (unsigned spin = 0; spin < (SPIN_MAX << 6); ++spin) {
    if (*(volatile LAS unsigned*)(L + M_FLAG) != 0u) break;
    __builtin_amdgcn_s_sleep(2);
  }
  asm volatile("" ::: "memory");
  STAMP(7);
  v4u gt[2] = {v4u{0u, 0u, 0u, 0u}, v4u{0u, 0u, 0u, 0u}};
  if (roleC) {
#pragma unroll
    for (int e = 0; e < 2; ++e) gt[e] = LD_SC1(WS_GATE + ((2 * w + e) * 64 + lane) * 16);
  }
  FENCE();
  const v4u qf0 = lds128(L + L_Q + (n & 3) * 256 + g * 32 + ((n >> 2) & 1) * 16), qf1 = lds128(L + L_Q + (n & 3) * 256 + 128 + g * 32 + ((n >> 2) & 1) * 16);
  float S[8][4];
#define QK(t) do { const f32x4 z_ = {0.f, 0.f, 0.f, 0.f}; \
    const f32x4 d0_ = mfma(kf[t][1], qf1, mfma(kf[t][0], qf0, z_)), d1_ = mfma(kf[t][3], qf1, mfma(kf[t][2], qf0, z_)); \
    _Pragma("unroll") for (int r_ = 0; r_ < 4; ++r_) { \
      auto s_ = __builtin_amdgcn_permlane32_swap(__float_as_uint(d0_[r_]), __float_as_uint(d1_[r_]), false, false); \
      S[t][r_] = __uint_as_float(s_[0]) + dpp<ROR4>(__uint_as_float(s_[1])); } } while (0)
  QK(0); QK(1); FENCE(); V_ISSUE(1);
  QK(2); QK(3); FENCE(); V_ISSUE(2);
  QK(4); QK(5); FENCE(); V_ISSUE(3);
  QK(6); QK(7); FENCE();
  STAMP(8);
  float mx = -__builtin_inff();
#pragma unroll
  for (int t = 0; t < 8; ++t)
#pragma unroll
    for (int r = 0; r < 4; r += 2) {
      const unsigned a = pk2(S[t][r], S[t][r + 1]);
      const unsigned b = pk2(bf_lo(a) * 0.08838834764831845f, bf_hi(a) * 0.08838834764831845f);
      S[t][r] = bf_lo(b); S[t][r + 1] = bf_hi(b);
      mx = fmaxf(mx, fmaxf(S[t][r], S[t][r + 1]));
    }
  mx = x32_max(x16_max(mx));
  float ls = 0.f;
  v4u pf[4];
#pragma unroll
  for (int u = 0; u < 4; ++u) {
    float e[8];
#pragma unroll
    for (int j = 0; j < 8; ++j) { e[j] = __builtin_amdgcn_exp2f((S[2 * u + (j >> 2)][j & 3] - mx) * LOG2E); ls += e[j]; }
    pf[u] = v4u{pk2(e[0], e[1]), pk2(e[2], e[3]), pk2(e[4], e[5]), pk2(e[6], e[7])};
  }
  ls = x32_sum(x16_sum(ls));
  if (lane < 4) *(LAS v2u*)(L + M_ML + w * 32 + lane * 8) = v2u{__float_as_uint(mx), __float_as_uint(ls)};
  if (roleC) {
    if (!all_lanes(umax4(gt[0]) != SENT && umax4(gt[1]) != SENT)) {
      for (unsigned spin = 0; spin < SPIN_MAX; ++spin) {
        asm volatile("" ::: "memory");
#pragma unroll
        for (int e = 0; e < 2; ++e) gt[e] = LD_SC1(WS_GATE + ((2 * w + e) * 64 + lane) * 16);
        if (all_lanes(umax4(gt[0]) != SENT && umax4(gt[1]) != SENT)) break;
      }
      asm volatile("s_waitcnt vmcnt(0)" ::: "memory");
    }
#pragma unroll
    for (int e = 0; e < 2; ++e) {
      const int gm = (2 * w + e) * 64 + lane;
#pragma unroll
      for (int b = 0; b < 4; ++b) *(LAS unsigned*)(L + L_GATE + b * 4096 + gm * 4) = gt[e][b];
    }
  }
  FENCE();
  STAMP(9);
  f32x4 oa[8];
#pragma unroll
  for (int d = 0; d < 8; ++d) oa[d] = f32x4{0.f, 0.f, 0.f, 0.f};
#pragma unroll
  for (int u = 0; u < 4; ++u) {
#pragma unroll
    for (int d = 0; d < 8; ++d) {
      const unsigned sel = (d & 1) ? 0x07060302u : 0x05040100u;
      v4u a;
#pragma unroll
      for (int i = 0; i < 4; ++i) a[i] = __builtin_amdgcn_perm(vf[u][2 * i + 1][d >> 1], vf[u][2 * i][d >> 1], sel);
      oa[d] = mfma(a, pf[u], oa[d]);
    }
    FENCE();
  }
  STAMP(10);
  int tid2 = threadIdx.x; asm volatile("" : "+v"(tid2));
#define tid tid2
  const int lane2 = tid2 & 63;
#define lane lane2
  const int g2 = lane2 >> 4, n2 = lane2 & 15;
#define g g2
#define n n2
  const int tb = tid2 >> 7, tk = 4 * (tid2 & 127);
  if (n < 4) {
#pragma unroll
    for (int r = 0; r < 4; ++r) {
      *(LAS f32x4*)(L + L_MRG + w * MRG_W + n * MRG_H + r * 128 + g * 32) = f32x4{oa[0][r], oa[1][r], oa[2][r], oa[3][r]};
      *(LAS f32x4*)(L + L_MRG + w * MRG_W + n * MRG_H + r * 128 + g * 32 + 16) = f32x4{oa[4][r], oa[5][r], oa[6][r], oa[7][r]};
    }
  }
  bar();
  if (w < 2) {
    const int h = tid >> 5, pos = 4 * (tid & 31);
    float mw[8], M = -__builtin_inff();
#pragma unroll
    for (int ww = 0; ww < 8; ++ww) { mw[ww] = *(LAS float*)(L + M_ML + ww * 32 + h * 8); M = fmaxf(M, mw[ww]); }
    f32x4 o = {0.f, 0.f, 0.f, 0.f}; float Ls = 0.f;
#pragma unroll
    for (int ww = 0; ww < 8; ++ww) {
      const float f = __builtin_amdgcn_exp2f((mw[ww] - M) * LOG2E);
      Ls += f * *(LAS float*)(L + M_ML + ww * 32 + h * 8 + 4);
      o += f * *(LAS f32x4*)(L + L_MRG + ww * MRG_W + h * MRG_H + pos * 4);
    }
    ST_SC1((v4u{nosent(o.x), nosent(o.y), nosent(o.z), nosent(o.w)}), WS_PART + cu * 2048 + h * 512 + pos * 4);
    if ((tid & 31) == 0) __builtin_amdgcn_raw_buffer_store_b64((v2u{nosent(M), nosent(Ls)}), rws, WS_STAT + cu * 32 + h * 8, 0, AUX_SC1);
  }
  STAMP(11);
  if (!(roleB || roleC)) {
    return;
  }
  asm volatile("s_barrier" ::: "memory");
  if (roleB) {
    {
      const int r0 = 64 * w + (lane >> 3);
      const GAS unsigned char* s = (const GAS unsigned char*)p.wo + ((size_t)(512 * X + r0) * 512 + 64 * (J - J_B) + 8 * img_chunk(lane, r0)) * 2;
#pragma unroll
      for (int e = 0; e < 8; ++e) glds16((const void*)(s + (size_t)e * 8 * 512 * 2), lds0 + L_W + w * 8192 + e * 1024);
    }
    const int bb = w >> 1, hf = lane >> 5;
    v4u po[2][4]; v2u st[2];
    for (unsigned spin = 0; spin < SPIN_MAX; ++spin) {
      unsigned m = 0;
#pragma unroll
      for (int hh = 0; hh < 2; ++hh) {
        const int lh = 2 * (w & 1) + hh;
#pragma unroll
        for (int i = 0; i < 4; ++i) { po[hh][i] = LD_SC1(WS_PART + ((8 * bb + 2 * i + hf) * 8 + X) * 2048 + lh * 512 + (lane & 31) * 16); m = max(m, umax4(po[hh][i])); }
        st[hh] = __builtin_amdgcn_raw_buffer_load_b64(rws, WS_STAT + ((8 * bb + 2 * (lane & 3) + hf) * 8 + X) * 32 + lh * 8, 0, AUX_SC1);
        m = max(m, max(st[hh].x, st[hh].y));
      }
      if (all_lanes(m != SENT)) break;
      asm volatile("" ::: "memory");
    }
    STAMP(12);
    asm volatile("s_waitcnt vmcnt(0)" ::: "memory");
    v4u bo[2][4];
#pragma unroll
    for (int ct = 0; ct < 4; ++ct) { lptr wp = L + L_W + w * 8192 + frag_base(lane, ct); bo[0][ct] = w_frag(wp, 0); bo[1][ct] = w_frag(wp, 1); }
    STAMP(19);
#pragma unroll
    for (int hh = 0; hh < 2; ++hh) {
      const float ms = __uint_as_float(st[hh].x);
      float M = fmaxf(ms, dpp<XOR1>(ms)); M = fmaxf(M, dpp<XOR2>(M)); M = x32_max(M);
      const float f = __builtin_amdgcn_exp2f((ms - M) * LOG2E);
      float Ls = f * __uint_as_float(st[hh].y); Ls += dpp<XOR1>(Ls); Ls += dpp<XOR2>(Ls); Ls = x32_sum(Ls);
      const float wt = f / Ls;
      const float w0 = dpp<0x00>(wt), w1 = dpp<0x55>(wt), w2 = dpp<0xAA>(wt), w3 = dpp<0xFF>(wt);
      float o[4];
#pragma unroll
      for (int e = 0; e < 4; ++e)
        o[e] = x32_sum(w0 * __uint_as_float(po[hh][0][e]) + w1 * __uint_as_float(po[hh][1][e]) + w2 * __uint_as_float(po[hh][2][e]) + w3 * __uint_as_float(po[hh][3][e]));
      const int pi = 4 * (lane & 31), dim = 32 * ((pi >> 3) & 3) + 8 * (pi >> 5) + (pi & 7);
      if (lane < 32) *(LAS v2u*)(L + L_ACT + bb * RS + act_off(128 * (2 * (w & 1) + hh) + dim)) = v2u{pk2(o[0], o[1]), pk2(o[2], o[3])};
    }
    STAMP(20);
    bar();
    STAMP(21);
    {
      lptr ap = L + L_ACT + (lane & 3) * RS + g * 16 + (2 * w) * 64;
      const v4u a0 = lds128(ap), a1 = lds128(ap + 64);
#pragma unroll
      for (int ct = 0; ct < 4; ++ct) {
        f32x4 d = mfma(a0, bo[0][ct], f32x4{0.f, 0.f, 0.f, 0.f});
        d = mfma(a1, bo[1][ct], d);
        *(LAS float*)(L + L_RED + w * 1024 + g * 256 + (16 * ct + n) * 4) = sel4(d, g);
      }
    }
    bar();
    STAMP(22);
    if (tid < 64) {
      f32x4 s = {0.f, 0.f, 0.f, 0.f};
#pragma unroll
      for (int ww = 0; ww < 8; ++ww) s += *(LAS f32x4*)(L + L_RED + ww * 1024 + tid * 16);
      ST_SC1((v4u{nosent(s.x), nosent(s.y), nosent(s.z), nosent(s.w)}), WS_ATT + ((X * 4 + g) * 512 + 64 * (J - J_B) + 4 * n) * 4);
    }
    STAMP(13);
    return;
  }
  const int pc = 7 * X + (J - J_C);
  {
    const int r0 = 64 * w + (lane >> 3), ch = img_chunk(lane, r0);
    const GAS unsigned char* s = (const GAS unsigned char*)((ch >> 2) ? p.wu : p.wg) + ((size_t)r0 * 1792 + 32 * pc + 8 * (ch & 3)) * 2;
#pragma unroll
    for (int e = 0; e < 8; ++e) glds16((const void*)(s + (size_t)e * 8 * 1792 * 2), lds0 + L_W + w * 8192 + e * 1024);
    const int rd = lane >> 3;
    const GAS unsigned char* sd = (const GAS unsigned char*)p.wd + ((size_t)(32 * pc + rd) * 512 + 64 * w + 8 * img_chunk(lane, rd)) * 2;
#pragma unroll
    for (int e = 0; e < 4; ++e) glds16((const void*)(sd + (size_t)e * 8 * 512 * 2), lds0 + L_WD + w * 4096 + e * 1024);
  }
  const v2u xr = *(const GAS v2u*)((const GAS unsigned char*)p.x + (tb * 512 + tk) * 2);
  STAMP(12); STAMP(13);
  {
    v4u at[8];
    for (unsigned spin = 0; spin < SPIN_MAX; ++spin) {
      unsigned m = 0;
#pragma unroll
      for (int i = 0; i < 8; ++i) { at[i] = LD_SC1(WS_ATT + ((i * 4 + tb) * 512 + tk) * 4); m = max(m, umax4(at[i])); }
      if (all_lanes(m != SENT)) break;
      asm volatile("" ::: "memory");
    }
    STAMP(14);
    asm volatile("s_waitcnt vmcnt(0)" ::: "memory");
    v4u bg[2][4], bd[4];
#pragma unroll
    for (int ct = 0; ct < 4; ++ct) { lptr wp = L + L_W + w * 8192 + frag_base(lane, ct); bg[0][ct] = w_frag(wp, 0); bg[1][ct] = w_frag(wp, 1); }
    {
      const int q = (lane >> 2) & 3;
#pragma unroll
      for (int kp = 0; kp < 4; ++kp) {
        const int ra = 8 * kp + q, rb_ = ra + 4;
        bd[kp] = tr_frag(L + L_WD + w * 4096 + ra * 128 + ((g ^ ((ra >> 1) & 3)) * 32) + (lane & 3) * 8, L + L_WD + w * 4096 + rb_ * 128 + ((g ^ ((rb_ >> 1) & 3)) * 32) + (lane & 3) * 8);
      }
    }
    STAMP(19);
    const v2u ga = *(LAS v2u*)(L + L_GATE + tb * 4096 + tk * 2), sb = *(LAS v2u*)(L + L_GATE + tb * 4096 + 1024 + tk * 2), cb = *(LAS v2u*)(L + L_GATE + tb * 4096 + 2048 + tk * 2);
    const float gav[4] = {bf_lo(ga.x), bf_hi(ga.x), bf_lo(ga.y), bf_hi(ga.y)}, sbv[4] = {bf_lo(sb.x), bf_hi(sb.x), bf_lo(sb.y), bf_hi(sb.y)},
                cbv[4] = {bf_lo(cb.x), bf_hi(cb.x), bf_lo(cb.y), bf_hi(cb.y)}, xs[4] = {bf_lo(xr.x), bf_hi(xr.x), bf_lo(xr.y), bf_hi(xr.y)};
    float x2[4];
#pragma unroll
    for (int e = 0; e < 4; ++e) {
      float a = 0.f;
#pragma unroll
      for (int i = 0; i < 8; ++i) a += __uint_as_float(at[i][e]);
      x2[e] = rb(xs[e] + rb(gav[e] * rb(a)));
    }
    *(LAS v2u*)(L + L_X + (tb * 512 + tk) * 2) = v2u{pk2(x2[0], x2[1]), pk2(x2[2], x2[3])};
    float sum, m2;
    ln_wave_stats(x2, sum, m2);
    if (lane == 0) *(LAS v2u*)(L + M_LN + w * 8) = v2u{__float_as_uint(sum), __float_as_uint(m2)};
    bar();
    STAMP(20);
    float mean_r, den_r;
    ln_row(L, tb, mean_r, den_r);
    float h2[4];
#pragma unroll
    for (int e = 0; e < 4; ++e) h2[e] = ln_mod(x2[e], mean_r, den_r, cbv[e], sbv[e]);
    *(LAS v2u*)(L + L_ACT + tb * RS + act_off(tk)) = v2u{pk2(h2[0], h2[1]), pk2(h2[2], h2[3])};
    bar();
    STAMP(21);
    {
      lptr ap = L + L_ACT + (lane & 3) * RS + g * 16 + (2 * w) * 64;
      const v4u a0 = lds128(ap), a1 = lds128(ap + 64);
#pragma unroll
      for (int ct = 0; ct < 4; ++ct) {
        f32x4 d = mfma(a0, bg[0][ct], f32x4{0.f, 0.f, 0.f, 0.f});
        d = mfma(a1, bg[1][ct], d);
        *(LAS float*)(L + L_RED + w * 1024 + g * 256 + (16 * ct + n) * 4) = sel4(d, g);
      }
    }
    bar();
    STAMP(22);
    if (w < 2) {
      const int b = 2 * w + (lane >> 5), un = lane & 31;
      float s = 0.f, up = 0.f;
#pragma unroll
      for (int ww = 0; ww < 8; ++ww) { s += *(LAS float*)(L + L_RED + ww * 1024 + b * 256 + un * 4); up += *(LAS float*)(L + L_RED + ww * 1024 + b * 256 + 128 + un * 4); }
      s = rb(s); up = rb(up);
      const float u = 0.7978845608028654f * (s + 0.044715f * s * s * s);
      const float ge = rb(s / (1.f + __builtin_amdgcn_exp2f(-2.f * LOG2E * u)));
      *(LAS unsigned short*)(L + M_HID + b * 64 + un * 2) = (unsigned short)(__float_as_uint(rb(ge * up)) >> 16);
    }
    bar();
    STAMP(23);
    f32x4 d = {0.f, 0.f, 0.f, 0.f};
#pragma unroll
    for (int kp = 0; kp < 4; ++kp) {
      v4u a4 = lds128(L + M_HID + (n & 3) * 64 + kp * 16);
      if ((n >> 2) != g) a4 = v4u{0u, 0u, 0u, 0u};
      d = mfma(a4, bd[kp], d);
    }
    ST_SC1((v4u{nosent(d[0]), nosent(d[1]), nosent(d[2]), nosent(d[3])}), WS_DOWN + pc * 8192 + (64 * w + lane) * 16);
  }
  STAMP(15);
  if (J < J_F_END) {
    const int f = 4 * X + (J - J_C);
    if (w < 7) {
      v4u dv[2];
      for (unsigned spin = 0; spin < SPIN_MAX; ++spin) {
#pragma unroll
        for (int i = 0; i < 2; ++i) dv[i] = LD_SC1(WS_DOWN + (28 * i + 4 * w + g) * 8192 + (16 * f + n) * 16);
        if (all_lanes(max(umax4(dv[0]), umax4(dv[1])) != SENT)) break;
        asm volatile("" ::: "memory");
      }
      f32x4 s;
#pragma unroll
      for (int b = 0; b < 4; ++b) s[b] = x32_sum(x16_sum(__uint_as_float(dv[0][b]) + __uint_as_float(dv[1][b])));
      if (lane < 16) *(LAS f32x4*)(L + L_RED + w * 256 + lane * 16) = s;
    }
    bar();
    if (tid < 64) {
      const int b = tid >> 4, col = 16 * f + n;
      float s = 0.f;
#pragma unroll
      for (int ww = 0; ww < 7; ++ww) s += *(LAS float*)(L + L_RED + ww * 256 + n * 16 + b * 4);
      const float y = rb(s);
      const float gb = bf_lo(*(LAS unsigned short*)(L + L_GATE + b * 4096 + 3072 + col * 2));
      const float x2 = bf_lo(*(LAS unsigned short*)(L + L_X + (b * 512 + col) * 2));
      const unsigned ob = pk2(x2 + rb(gb * y), 0.f) & 0xffffu;
      const unsigned o2 = ob | (dppu<XOR1>(ob) << 16);
      if ((n & 1) == 0) *(GAS unsigned*)((GAS unsigned char*)p.out + (b * 512 + col) * 2) = o2;
    }
  }
#undef tid
#undef lane
#undef g
#undef n
}
}

extern "C" void kernel_launch(void* const* d_in, const int* in_sizes, int n_in, void* d_out, int out_size, void* d_ws, size_t ws_size, hipStream_t stream) {
  (void)in_sizes; (void)n_in; (void)out_size; (void)ws_size;
  Params p;
  p.x = d_in[0]; p.k = d_in[1]; p.v = d_in[2]; p.wq = d_in[3]; p.wo = d_in[4];
  p.wg = d_in[6]; p.wu = d_in[7]; p.wd = d_in[8]; p.c = d_in[9]; p.wm = d_in[10];
  p.out = d_out; p.ws = d_ws;
  hipLaunchKernelGGL(adaln_block_kernel, dim3(NCU), dim3(NTHR), 0, stream, p);
}
```

```cpp
#include <hip/hip_runtime.h>
#include <hip/hip_bf16.h>
#include <stdint.h>

#define GAS __attribute__((address_space(1)))
#define LAS __attribute__((address_space(3)))
typedef unsigned v4u __attribute__((ext_vector_type(4)));
typedef unsigned v2u __attribute__((ext_vector_type(2)));
typedef float f32x4 __attribute__((ext_vector_type(4)));
typedef short s16x4 __attribute__((ext_vector_type(4)));
typedef __bf16 bf16x8 __attribute__((ext_vector_type(8)));
typedef __bf16 bf16x2 __attribute__((ext_vector_type(2)));
typedef LAS unsigned char* lptr;

#define PACE 4
#define STAMPS 0

namespace {
constexpr int NCU = 256, NTHR = 512;
constexpr unsigned KV_BYTES = 4u * 8u * 8192u * 128u * 2u;
constexpr unsigned WS_EPOCH = 0, WS_READY = 4096, WS_H = 8192, WS_Q = 16384, WS_GATE = WS_Q + 65536, WS_STAT = WS_GATE + 16384, WS_ATT = WS_STAT + 8192,
                   WS_PART = WS_ATT + 65536, WS_DOWN = WS_PART + 256 * 2048, WS_END = WS_DOWN + 56 * 8192, WS_STAMP = WS_END + 65536;
constexpr int RS = 1088;
constexpr int L_W = 0, L_WD = 65536, L_MRG = 98304, L_GATE = L_MRG + 17408, L_ACT = L_GATE + 16384, L_X = L_ACT + 4352, L_Q = L_X + 4096, L_RED = L_Q + 1024,
              L_MISC = L_RED + 8192, L_BYTES = L_MISC + 1024;
constexpr int MRG_H = 528, MRG_W = 4 * MRG_H;
constexpr int M_LN = L_MISC, M_ML = L_MISC + 64, M_HID = L_MISC + 320, M_FLAG = L_MISC + 576;
constexpr unsigned SENT = 0xFFFFFFFFu;
constexpr unsigned SPIN_MAX = 1u << 15;
constexpr int AUX_NT = 2, AUX_SC1 = 16;
constexpr float LOG2E = 1.4426950408889634f;
constexpr int J_H = 0, J_G = 2, J_Q = 6, J_B = 14, J_C = 22, J_F_END = 26, J_C_END = 29;

struct Params { const void* x; const void* k; const void* v; const void* wq; const void* wo; const void* wg; const void* wu; const void* wd; const void* c; const void* wm; void* out; void* ws; };

__device__ __forceinline__ float bf_lo(unsigned u) { return __builtin_bit_cast(float, u << 16); }
__device__ __forceinline__ float bf_hi(unsigned u) { return __builtin_bit_cast(float, u & 0xffff0000u); }
__device__ __forceinline__ unsigned pk2(float a, float b) { bf16x2 r = {(__bf16)a, (__bf16)b}; return __builtin_bit_cast(unsigned, r); }
__device__ __forceinline__ float rb(float a) { return bf_lo(pk2(a, a)); }
template <int CTRL> __device__ __forceinline__ float dpp(float x) { return __builtin_bit_cast(float, __builtin_amdgcn_mov_dpp(__builtin_bit_cast(int, x), CTRL, 0xf, 0xf, true)); }
template <int CTRL> __device__ __forceinline__ unsigned dppu(unsigned x) { return (unsigned)__builtin_amdgcn_mov_dpp((int)x, CTRL, 0xf, 0xf, true); }
constexpr int XOR1 = 0xB1, XOR2 = 0x4E, HMIR = 0x141, ROR4 = 0x124, ROR8 = 0x128;
__device__ __forceinline__ float x16_sum(float x) { auto s = __builtin_amdgcn_permlane16_swap(__float_as_uint(x), __float_as_uint(x), false, false); return __uint_as_float(s[0]) + __uint_as_float(s[1]); }
__device__ __forceinline__ float x32_sum(float x) { auto s = __builtin_amdgcn_permlane32_swap(__float_as_uint(x), __float_as_uint(x), false, false); return __uint_as_float(s[0]) + __uint_as_float(s[1]); }
__device__ __forceinline__ float x16_max(float x) { auto s = __builtin_amdgcn_permlane16_swap(__float_as_uint(x), __float_as_uint(x), false, false); return fmaxf(__uint_as_float(s[0]), __uint_as_float(s[1])); }
__device__ __forceinline__ float x32_max(float x) { auto s = __builtin_amdgcn_permlane32_swap(__float_as_uint(x), __float_as_uint(x), false, false); return fmaxf(__uint_as_float(s[0]), __uint_as_float(s[1])); }
__device__ __forceinline__ float wave_sum(float x) { x += dpp<XOR1>(x); x += dpp<XOR2>(x); x += dpp<HMIR>(x); x += dpp<ROR8>(x); return x32_sum(x16_sum(x)); }
__device__ __forceinline__ f32x4 mfma(v4u a, v4u b, f32x4 c) { return __builtin_amdgcn_mfma_f32_16x16x32_bf16(__builtin_bit_cast(bf16x8, a), __builtin_bit_cast(bf16x8, b), c, 0, 0, 0); }
__device__ __forceinline__ v2u tr_read(lptr p) { s16x4 r = __builtin_amdgcn_ds_read_tr16_b64_v4i16((LAS s16x4*)p); return __builtin_bit_cast(v2u, r); }
__device__ __forceinline__ v4u tr_frag(lptr p0, lptr p1) { v2u a = tr_read(p0), b = tr_read(p1); return v4u{a.x, a.y, b.x, b.y}; }
__device__ __forceinline__ v4u lds128(lptr p) { return *(LAS v4u*)p; }
__device__ __forceinline__ void bar() { asm volatile("s_waitcnt lgkmcnt(0)\n\ts_barrier" ::: "memory"); }
__device__ __forceinline__ void glds16(const void* gsrc, unsigned lds_dst) {
  unsigned keep;
  asm volatile("s_mov_b32 %0, m0\n\ts_mov_b32 m0, %2\n\ts_nop 0\n\tglobal_load_lds_dwordx4 %1, off nt\n\ts_mov_b32 m0, %0" : "=&s"(keep) : "v"(gsrc), "s"(lds_dst) : "memory");
}
__device__ __forceinline__ unsigned nosent(float f) { unsigned u = __float_as_uint(f); return u == SENT ? SENT - 1u : u; }
__device__ __forceinline__ unsigned nosentu(unsigned u) { return u == SENT ? SENT - 1u : u; }
__device__ __forceinline__ unsigned umax4(v4u a) { return max(max(a.x, a.y), max(a.z, a.w)); }
__device__ __forceinline__ bool all_lanes(bool ok) { return __builtin_amdgcn_ballot_w64(!ok) == 0ull; }
__device__ __forceinline__ float sel4(f32x4 d, int g) { return g == 0 ? d[0] : g == 1 ? d[1] : g == 2 ? d[2] : d[3]; }
#define RSRC(p, n) __builtin_amdgcn_make_buffer_rsrc((void*)(p), 0, (int)(n), 0x00020000)
#define LD_SC1(off) __builtin_amdgcn_raw_buffer_load_b128(rws, (off), 0, AUX_SC1)
#define ST_SC1(v, off) __builtin_amdgcn_raw_buffer_store_b128((v), rws, (off), 0, AUX_SC1)
#define FENCE() __builtin_amdgcn_sched_barrier(0)
#define STAMP(i) do { } while (0)

__device__ __forceinline__ int img_chunk(int lane, int row) { return ((((lane & 7) >> 1) ^ ((row >> 1) & 3)) << 1) | (lane & 1); }
__device__ __forceinline__ int frag_base(int lane, int ct) { const int g = lane >> 4, q = (lane >> 2) & 3; return (4 * g + q) * 128 + ((ct ^ ((2 * g + (q >> 1)) & 3)) * 32) + (lane & 3) * 8; }
__device__ __forceinline__ v4u w_frag(lptr base, int s) { return tr_frag(base + s * 4096, base + s * 4096 + 2048); }
__device__ __forceinline__ int act_off(int k) { return (k >> 5) * 64 + ((k >> 2) & 3) * 16 + ((k >> 4) & 1) * 8; }
__device__ __forceinline__ f32x4 gemv_tile(lptr W, lptr act, int lane, int ct) {
  lptr ap = act + (lane & 3) * RS + (lane >> 4) * 16;
  lptr wp = W + frag_base(lane, ct);
  f32x4 a0 = {0.f, 0.f, 0.f, 0.f}, a1 = {0.f, 0.f, 0.f, 0.f};
#pragma unroll
  for (int s = 0; s < 16; s += 2) {
    a0 = mfma(lds128(ap + s * 64), w_frag(wp, s), a0);
    a1 = mfma(lds128(ap + s * 64 + 64), w_frag(wp, s + 1), a1);
  }
  return a0 + a1;
}
__device__ __forceinline__ void ln_wave_stats(const float (&v)[4], float& sum, float& m2) {
  sum = wave_sum((v[0] + v[1]) + (v[2] + v[3]));
  const float mw = sum * (1.f / 256.f);
  const float d0 = v[0] - mw, d1 = v[1] - mw, d2 = v[2] - mw, d3 = v[3] - mw;
  m2 = wave_sum((d0 * d0 + d1 * d1) + (d2 * d2 + d3 * d3));
}
__device__ __forceinline__ void ln_row(lptr L, int b, float& mean_r, float& den_r) {
  const f32x4 s = *(LAS f32x4*)(L + M_LN + b * 16);
  const float dm = (s.x - s.z) * (1.f / 256.f);
  const float mean = (s.x + s.z) * (1.f / 512.f);
  const float var = (s.y + s.w + dm * dm * 128.f) * (1.f / 512.f);
  mean_r = rb(mean);
  den_r = rb(__builtin_sqrtf(rb(rb(var) + 1e-5f)));
}
__device__ __forceinline__ float ln_mod(float x, float mean_r, float den_r, float cc, float ss) {
  const float ln = rb(rb(x - mean_r) / den_r);
  return rb(rb(ln * rb(1.f + cc)) + ss);
}

__global__ void __launch_bounds__(NTHR, 2) adaln_block_kernel(Params p) {
  __shared__ __attribute__((aligned(16))) unsigned char smem[L_BYTES + (STAMPS ? 1024 : 0)];
  lptr L = (lptr)smem;
  const unsigned lds0 = (unsigned)(uintptr_t)L;
  const int tid = threadIdx.x, lane = tid & 63, w = __builtin_amdgcn_readfirstlane(tid >> 6);
  const int cu = blockIdx.x, X = cu & 7, J = cu >> 3;
  const int g = lane >> 4, n = lane & 15;
  const auto rws = RSRC(p.ws, WS_END);
  const auto rk = RSRC(p.k, KV_BYTES);
  const auto rv = RSRC(p.v, KV_BYTES);
  const bool roleH = J < J_G, roleG = J >= J_G && J < J_Q, roleM = J < J_Q, roleQ = J >= J_Q && J < J_B, roleB = J >= J_B && J < J_C, roleC = J >= J_C && J < J_C_END;
  STAMP(0);
  if (w == 3) {
    const GAS unsigned char* a;
    const int i = lane & 15;
    if (i < 4) a = (const GAS unsigned char*)p.wo + (size_t)i * (1u << 20);
    else if (i < 6) a = (const GAS unsigned char*)p.wg + (size_t)(i - 4) * (1u << 20);
    else if (i < 8) a = (const GAS unsigned char*)p.wu + (size_t)(i - 6) * (1u << 20);
    else if (i < 10) a = (const GAS unsigned char*)p.wd + (size_t)(i - 8) * (1u << 20);
    else if (i < 11) a = (const GAS unsigned char*)p.out;
    else a = (const GAS unsigned char*)p.v + ((((unsigned)(J >> 3) * 8u + (unsigned)X) * 8192u + (unsigned)(J & 7) * 1024u) * 256u) + (size_t)(i - 11) * 49152;
    glds16((const void*)a, lds0 + L_RED + 4096);
  }
  const unsigned long long ep = __hip_atomic_load((GAS unsigned long long*)p.ws, __ATOMIC_RELAXED, __HIP_MEMORY_SCOPE_AGENT);
  v2u cv = {0u, 0u}, xv = {0u, 0u};
  if (roleM) {
    cv = *(const GAS v2u*)((const GAS unsigned char*)p.c + ((tid >> 7) * 512 + 4 * (tid & 127)) * 2);
    if (roleH) xv = *(const GAS v2u*)((const GAS unsigned char*)p.x + ((tid >> 7) * 512 + 4 * (tid & 127)) * 2);
  }
  if (roleM || roleQ) {
    const int r0 = 64 * w + (lane >> 3), ch = img_chunk(lane, r0);
    const GAS unsigned char* s;
    size_t rstride;
    if (roleH) { s = (const GAS unsigned char*)p.wm + ((size_t)r0 * 3072 + (ch >> 2) * 512 + 32 * (2 * X + J) + 8 * (ch & 3)) * 2; rstride = 3072 * 2; }
    else if (roleG) { s = (const GAS unsigned char*)p.wm + ((size_t)r0 * 3072 + 1024 + 64 * (4 * X + (J - J_G)) + 8 * ch) * 2; rstride = 3072 * 2; }
    else { s = (const GAS unsigned char*)p.wq + ((size_t)r0 * 4096 + 64 * (8 * X + (J - J_Q)) + 8 * ch) * 2; rstride = 4096 * 2; }
#pragma unroll
    for (int e = 0; e < 8; ++e) glds16((const void*)(s + (size_t)e * 8 * rstride), lds0 + L_W + w * 8192 + e * 1024);
    asm volatile("s_barrier" ::: "memory");
  }
  if (w == 1) {
    const v4u sent = {SENT, SENT, SENT, SENT};
    ST_SC1(sent, WS_PART + cu * 2048 + lane * 16);
    ST_SC1(sent, WS_PART + cu * 2048 + 1024 + lane * 16);
    if (lane < 2) ST_SC1(sent, WS_STAT + cu * 32 + lane * 16);
    if (roleB) ST_SC1(sent, WS_ATT + ((X * 4 + g) * 512 + 64 * (J - J_B) + 4 * n) * 4);
    if (roleG && lane < 32) ST_SC1(sent, WS_GATE + (4 * X + (J - J_G)) * 512 + lane * 16);
    if (roleC) {
#pragma unroll
      for (int e = 0; e < 8; ++e) ST_SC1(sent, WS_DOWN + (7 * X + (J - J_C)) * 8192 + e * 1024 + lane * 16);
    }
  }
  FENCE();
  const unsigned kvbase = (((unsigned)(J >> 3) * 8u + (unsigned)X) * 8192u + (unsigned)(J & 7) * 1024u) * 256u + (unsigned)w * 4096u;
  const unsigned koff = (4 * (lane & 3) + ((lane >> 2) & 1)) * 256 + (lane >> 3) * 16;
  v4u kf[8][4];
#define K_ISSUE(t) do { unsigned ko_ = koff; asm volatile("" : "+v"(ko_)); _Pragma("unroll") for (int q_ = 0; q_ < 4; ++q_) kf[t][q_] = __builtin_amdgcn_raw_buffer_load_b128(rk, ko_ + (q_ >> 1) * 512 + (q_ & 1) * 128, kvbase + (t) * 32768, AUX_NT); FENCE(); } while (0)
  K_ISSUE(0); K_ISSUE(1);
  if (tid == 0) *(LAS unsigned*)(L + M_FLAG) = 0u;
  const unsigned long long E = ep + 1ull;
  const unsigned ta = __builtin_amdgcn_readfirstlane((unsigned)E), tb2 = __builtin_amdgcn_readfirstlane(((unsigned)E * 0x9E3779B1u) ^ (unsigned)(E >> 32));
  if (roleM) {
    const int tb = tid >> 7, tk = 4 * (tid & 127);
    float cs[4] = {bf_lo(cv.x), bf_hi(cv.x), bf_lo(cv.y), bf_hi(cv.y)};
#pragma unroll
    for (int e = 0; e < 4; ++e) cs[e] = cs[e] / (1.f + __expf(-cs[e]));
    *(LAS v2u*)(L + L_ACT + tb * RS + act_off(tk)) = v2u{pk2(cs[0], cs[1]), pk2(cs[2], cs[3])};
    if (roleH) {
      *(LAS v2u*)(L + L_X + (tb * 512 + tk) * 2) = xv;
      const float xs[4] = {bf_lo(xv.x), bf_hi(xv.x), bf_lo(xv.y), bf_hi(xv.y)};
      float sum, m2;
      ln_wave_stats(xs, sum, m2);
      if (lane == 0) *(LAS v2u*)(L + M_LN + w * 8) = v2u{__float_as_uint(sum), __float_as_uint(m2)};
    }
  }
  STAMP(17);
  if (roleM || roleQ) asm volatile("s_waitcnt vmcnt(8)" ::: "memory");
  STAMP(18);
  bar();
  STAMP(1);
  if (roleH && w < 2) {
    const float ss = rb(sel4(gemv_tile(L + L_W, L + L_ACT, lane, w), g));
    const float cc = rb(sel4(gemv_tile(L + L_W, L + L_ACT, lane, w + 2), g));
    float mean_r, den_r;
    ln_row(L, g, mean_r, den_r);
    const int col = 32 * (2 * X + J) + 16 * w + n;
    const float xx = bf_lo(*(LAS unsigned short*)(L + L_X + (g * 512 + col) * 2));
    const unsigned hb = __float_as_uint(ln_mod(xx, mean_r, den_r, cc, ss)) >> 16;
    const unsigned d0 = dppu<0x00>(hb) | (dppu<0x55>(hb) << 16), d1 = dppu<0xAA>(hb) | (dppu<0xFF>(hb) << 16);
    if ((n & 3) == 0) ST_SC1((v4u{d0, ta, d1, tb2}), WS_H + (col >> 3) * 128 + (g * 2 + ((n >> 2) & 1)) * 16);
    FENCE();
  }
  if (roleG && w < 4) {
    const f32x4 d = gemv_tile(L + L_W, L + L_ACT, lane, w);
    unsigned o[4];
#pragma unroll
    for (int b = 0; b < 4; ++b) { const unsigned hb = __float_as_uint(rb(d[b])) >> 16; o[b] = nosentu(hb | (dppu<XOR1>(hb) << 16)); }
    if (lane < 16 && (lane & 1) == 0) *(LAS v4u*)(L + L_RED + (8 * w + (lane >> 1)) * 16) = v4u{o[0], o[1], o[2], o[3]};
    FENCE();
  }
  if (roleG) bar();
  STAMP(2);
#define PACE_WAIT() do { if (PACE >= 0) asm volatile("s_waitcnt vmcnt(%0)" ::"i"(PACE < 0 ? 0 : PACE) : "memory"); } while (0)
  if (w == 1) {
    asm volatile("s_waitcnt vmcnt(8)" ::: "memory");
    if (roleG && lane < 32) ST_SC1(lds128(L + L_RED + lane * 16), WS_GATE + (4 * X + (J - J_G)) * 512 + lane * 16);
    if (lane == 0) ST_SC1((v4u{0u, ta, 0u, tb2}), WS_READY + cu * 16);
    FENCE();
  }
  PACE_WAIT(); K_ISSUE(2); PACE_WAIT(); K_ISSUE(3);
  if (w == 0 && roleQ) {
    v4u hv[8];
    for (unsigned spin = 0; spin < SPIN_MAX; ++spin) {
      bool ok = true;
#pragma unroll
      for (int i = 0; i < 8; ++i) { hv[i] = LD_SC1(WS_H + (64 * i + lane) * 16); ok &= (hv[i].y == ta) & (hv[i].w == tb2); }
      if (all_lanes(ok)) break;
      asm volatile("" ::: "memory");
    }
    STAMP(3);
#pragma unroll
    for (int i = 0; i < 8; ++i) { const int gm = 64 * i + lane; *(LAS v2u*)(L + L_ACT + ((gm >> 1) & 3) * RS + act_off(8 * (gm >> 3) + 4 * (gm & 1))) = v2u{hv[i].x, hv[i].z}; }
    asm volatile("s_waitcnt lgkmcnt(0)" ::: "memory");
#pragma unroll
    for (int ct = 0; ct < 4; ++ct) {
      const f32x4 d = gemv_tile(L + L_W, L + L_ACT, lane, ct);
      const unsigned qb = __float_as_uint(rb(sel4(d, g))) >> 16;
      const unsigned d0 = dppu<0x00>(qb) | (dppu<0x55>(qb) << 16), d1 = dppu<0xAA>(qb) | (dppu<0xFF>(qb) << 16);
      if ((n & 3) == 0) ST_SC1((v4u{d0, ta, d1, tb2}), WS_Q + (8 * X + (J - J_Q)) * 1024 + (g * 16 + 4 * ct + (n >> 2)) * 16);
    }
    FENCE();
    STAMP(4);
  }
  PACE_WAIT(); K_ISSUE(4); PACE_WAIT(); K_ISSUE(5);
  if (w == 0) {
    v4u qv[2];
    const bool need_ready = roleB || roleC;
    for (unsigned spin = 0; spin < SPIN_MAX; ++spin) {
      bool ok = true;
#pragma unroll
      for (int i = 0; i < 2; ++i) { const int gm = 64 * i + lane; qv[i] = LD_SC1(WS_Q + (8 * X + (gm >> 4)) * 1024 + ((J >> 3) * 16 + (gm & 15)) * 16); ok &= (qv[i].y == ta) & (qv[i].w == tb2); }
      if (need_ready) {
#pragma unroll
        for (int i = 0; i < 4; ++i) { const v4u rd = LD_SC1(WS_READY + (64 * i + lane) * 16); ok &= (rd.y == ta) & (rd.w == tb2); }
      }
      if (all_lanes(ok)) break;
      asm volatile("" ::: "memory");
    }
#pragma unroll
    for (int i = 0; i < 2; ++i) *(LAS v2u*)(L + L_Q + (64 * i + lane) * 8) = v2u{qv[i].x, qv[i].z};
    asm volatile("s_waitcnt lgkmcnt(0)" ::: "memory");
    if (lane == 0) *(volatile LAS unsigned*)(L + M_FLAG) = 1u;
    if (cu == 8 * J_B && lane == 0) __hip_atomic_store((GAS unsigned long long*)p.ws, E, __ATOMIC_RELAXED, __HIP_MEMORY_SCOPE_AGENT);
    FENCE();
    STAMP(5);
  }
  PACE_WAIT(); K_ISSUE(6); PACE_WAIT(); K_ISSUE(7);
  v4u vf[4][8];
#define V_ISSUE(u) do { unsigned vo_ = lane * 16; asm volatile("" : "+v"(vo_)); _Pragma("unroll") for (int j_ = 0; j_ < 8; ++j_) vf[u][j_] = __builtin_amdgcn_raw_buffer_load_b128(rv, vo_ + (j_ & 3) * 1024, kvbase + (2 * (u) + (j_ >> 2)) * 32768, AUX_NT); FENCE(); } while (0)
  PACE_WAIT(); V_ISSUE(0);
  STAMP(6);
  for (unsigned spin = 0; spin < (SPIN_MAX << 6); ++spin) {
    if (*(volatile LAS unsigned*)(L + M_FLAG) != 0u) break;
    __builtin_amdgcn_s_sleep(2);
  }
  asm volatile("" ::: "memory");
  STAMP(7);
  v4u gt[2] = {v4u{0u, 0u, 0u, 0u}, v4u{0u, 0u, 0u, 0u}};
  if (roleC) {
#pragma unroll
    for (int e = 0; e < 2; ++e) gt[e] = LD_SC1(WS_GATE + ((2 * w + e) * 64 + lane) * 16);
  }
  FENCE();
  const v4u qf0 = lds128(L + L_Q + (n & 3) * 256 + g * 32 + ((n >> 2) & 1) * 16), qf1 = lds128(L + L_Q + (n & 3) * 256 + 128 + g * 32 + ((n >> 2) & 1) * 16);
  float S[8][4];
#define QK(t) do { const f32x4 z_ = {0.f, 0.f, 0.f, 0.f}; \
    const f32x4 d0_ = mfma(kf[t][1], qf1, mfma(kf[t][0], qf0, z_)), d1_ = mfma(kf[t][3], qf1, mfma(kf[t][2], qf0, z_)); \
    _Pragma("unroll") for (int r_ = 0; r_ < 4; ++r_) { \
      auto s_ = __builtin_amdgcn_permlane32_swap(__float_as_uint(d0_[r_]), __float_as_uint(d1_[r_]), false, false); \
      S[t][r_] = __uint_as_float(s_[0]) + dpp<ROR4>(__uint_as_float(s_[1])); } } while (0)
  QK(0); QK(1); FENCE(); V_ISSUE(1);
  QK(2); QK(3); FENCE(); V_ISSUE(2);
  QK(4); QK(5); FENCE(); V_ISSUE(3);
  QK(6); QK(7); FENCE();
  STAMP(8);
  float mx = -__builtin_inff();
#pragma unroll
  for (int t = 0; t < 8; ++t)
#pragma unroll
    for (int r = 0; r < 4; r += 2) {
      const unsigned a = pk2(S[t][r], S[t][r + 1]);
      const unsigned b = pk2(bf_lo(a) * 0.08838834764831845f, bf_hi(a) * 0.08838834764831845f);
      S[t][r] = bf_lo(b); S[t][r + 1] = bf_hi(b);
      mx = fmaxf(mx, fmaxf(S[t][r], S[t][r + 1]));
    }
  mx = x32_max(x16_max(mx));
  float ls = 0.f;
  v4u pf[4];
#pragma unroll
  for (int u = 0; u < 4; ++u) {
    float e[8];
#pragma unroll
    for (int j = 0; j < 8; ++j) { e[j] = __builtin_amdgcn_exp2f((S[2 * u + (j >> 2)][j & 3] - mx) * LOG2E); ls += e[j]; }
    pf[u] = v4u{pk2(e[0], e[1]), pk2(e[2], e[3]), pk2(e[4], e[5]), pk2(e[6], e[7])};
  }
  ls = x32_sum(x16_sum(ls));
  if (lane < 4) *(LAS v2u*)(L + M_ML + w * 32 + lane * 8) = v2u{__float_as_uint(mx), __float_as_uint(ls)};
  if (roleC) {
    if (!all_lanes(umax4(gt[0]) != SENT && umax4(gt[1]) != SENT)) {
      for (unsigned spin = 0; spin < SPIN_MAX; ++spin) {
        asm volatile("" ::: "memory");
#pragma unroll
        for (int e = 0; e < 2; ++e) gt[e] = LD_SC1(WS_GATE + ((2 * w + e) * 64 + lane) * 16);
        if (all_lanes(umax4(gt[0]) != SENT && umax4(gt[1]) != SENT)) break;
      }
      asm volatile("s_waitcnt vmcnt(0)" ::: "memory");
    }
#pragma unroll
    for (int e = 0; e < 2; ++e) {
      const int gm = (2 * w + e) * 64 + lane;
#pragma unroll
      for (int b = 0; b < 4; ++b) *(LAS unsigned*)(L + L_GATE + b * 4096 + gm * 4) = gt[e][b];
    }
  }
  FENCE();
  STAMP(9);
  f32x4 oa[8];
#pragma unroll
  for (int d = 0; d < 8; ++d) oa[d] = f32x4{0.f, 0.f, 0.f, 0.f};
#pragma unroll
  for (int u = 0; u < 4; ++u) {
#pragma unroll
    for (int d = 0; d < 8; ++d) {
      const unsigned sel = (d & 1) ? 0x07060302u : 0x05040100u;
      v4u a;
#pragma unroll
      for (int i = 0; i < 4; ++i) a[i] = __builtin_amdgcn_perm(vf[u][2 * i + 1][d >> 1], vf[u][2 * i][d >> 1], sel);
      oa[d] = mfma(a, pf[u], oa[d]);
    }
    FENCE();
  }
  STAMP(10);
  int tid2 = threadIdx.x; asm volatile("" : "+v"(tid2));
#define tid tid2
  const int lane2 = tid2 & 63;
#define lane lane2
  const int g2 = lane2 >> 4, n2 = lane2 & 15;
#define g g2
#define n n2
  const int tb = tid2 >> 7, tk = 4 * (tid2 & 127);
  if (n < 4) {
#pragma unroll
    for (int r = 0; r < 4; ++r) {
      *(LAS f32x4*)(L + L_MRG + w * MRG_W + n * MRG_H + r * 128 + g * 32) = f32x4{oa[0][r], oa[1][r], oa[2][r], oa[3][r]};
      *(LAS f32x4*)(L + L_MRG + w * MRG_W + n * MRG_H + r * 128 + g * 32 + 16) = f32x4{oa[4][r], oa[5][r], oa[6][r], oa[7][r]};
    }
  }
  bar();
  if (w < 2) {
    const int h = tid >> 5, pos = 4 * (tid & 31);
    float mw[8], M = -__builtin_inff();
#pragma unroll
    for (int ww = 0; ww < 8; ++ww) { mw[ww] = *(LAS float*)(L + M_ML + ww * 32 + h * 8); M = fmaxf(M, mw[ww]); }
    f32x4 o = {0.f, 0.f, 0.f, 0.f}; float Ls = 0.f;
#pragma unroll
    for (int ww = 0; ww < 8; ++ww) {
      const float f = __builtin_amdgcn_exp2f((mw[ww] - M) * LOG2E);
      Ls += f * *(LAS float*)(L + M_ML + ww * 32 + h * 8 + 4);
      o += f * *(LAS f32x4*)(L + L_MRG + ww * MRG_W + h * MRG_H + pos * 4);
    }
    ST_SC1((v4u{nosent(o.x), nosent(o.y), nosent(o.z), nosent(o.w)}), WS_PART + cu * 2048 + h * 512 + pos * 4);
    if ((tid & 31) == 0) __builtin_amdgcn_raw_buffer_store_b64((v2u{nosent(M), nosent(Ls)}), rws, WS_STAT + cu * 32 + h * 8, 0, AUX_SC1);
  }
  STAMP(11);
  if (!(roleB || roleC)) {
    return;
  }
  asm volatile("s_barrier" ::: "memory");
  if (roleB) {
    {
      const int r0 = 64 * w + (lane >> 3);
      const GAS unsigned char* s = (const GAS unsigned char*)p.wo + ((size_t)(512 * X + r0) * 512 + 64 * (J - J_B) + 8 * img_chunk(lane, r0)) * 2;
#pragma unroll
      for (int e = 0; e < 8; ++e) glds16((const void*)(s + (size_t)e * 8 * 512 * 2), lds0 + L_W + w * 8192 + e * 1024);
    }
    const int bb = w >> 1, hf = lane >> 5;
    v4u po[2][4]; v2u st[2];
    for (unsigned spin = 0; spin < SPIN_MAX; ++spin) {
      unsigned m = 0;
#pragma unroll
      for (int hh = 0; hh < 2; ++hh) {
        const int lh = 2 * (w & 1) + hh;
#pragma unroll
        for (int i = 0; i < 4; ++i) { po[hh][i] = LD_SC1(WS_PART + ((8 * bb + 2 * i + hf) * 8 + X) * 2048 + lh * 512 + (lane & 31) * 16); m = max(m, umax4(po[hh][i])); }
        st[hh] = __builtin_amdgcn_raw_buffer_load_b64(rws, WS_STAT + ((8 * bb + 2 * (lane & 3) + hf) * 8 + X) * 32 + lh * 8, 0, AUX_SC1);
        m = max(m, max(st[hh].x, st[hh].y));
      }
      if (all_lanes(m != SENT)) break;
      asm volatile("" ::: "memory");
    }
    STAMP(12);
    asm volatile("s_waitcnt vmcnt(0)" ::: "memory");
    v4u bo[2][4];
#pragma unroll
    for (int ct = 0; ct < 4; ++ct) { lptr wp = L + L_W + w * 8192 + frag_base(lane, ct); bo[0][ct] = w_frag(wp, 0); bo[1][ct] = w_frag(wp, 1); }
    STAMP(19);
#pragma unroll
    for (int hh = 0; hh < 2; ++hh) {
      const float ms = __uint_as_float(st[hh].x);
      float M = fmaxf(ms, dpp<XOR1>(ms)); M = fmaxf(M, dpp<XOR2>(M)); M = x32_max(M);
      const float f = __builtin_amdgcn_exp2f((ms - M) * LOG2E);
      float Ls = f * __uint_as_float(st[hh].y); Ls += dpp<XOR1>(Ls); Ls += dpp<XOR2>(Ls); Ls = x32_sum(Ls);
      const float wt = f / Ls;
      const float w0 = dpp<0x00>(wt), w1 = dpp<0x55>(wt), w2 = dpp<0xAA>(wt), w3 = dpp<0xFF>(wt);
      float o[4];
#pragma unroll
      for (int e = 0; e < 4; ++e)
        o[e] = x32_sum(w0 * __uint_as_float(po[hh][0][e]) + w1 * __uint_as_float(po[hh][1][e]) + w2 * __uint_as_float(po[hh][2][e]) + w3 * __uint_as_float(po[hh][3][e]));
      const int pi = 4 * (lane & 31), dim = 32 * ((pi >> 3) & 3) + 8 * (pi >> 5) + (pi & 7);
      if (lane < 32) *(LAS v2u*)(L + L_ACT + bb * RS + act_off(128 * (2 * (w & 1) + hh) + dim)) = v2u{pk2(o[0], o[1]), pk2(o[2], o[3])};
    }
    STAMP(20);
    bar();
    STAMP(21);
    {
      lptr ap = L + L_ACT + (lane & 3) * RS + g * 16 + (2 * w) * 64;
      const v4u a0 = lds128(ap), a1 = lds128(ap + 64);
#pragma unroll
      for (int ct = 0; ct < 4; ++ct) {
        f32x4 d = mfma(a0, bo[0][ct], f32x4{0.f, 0.f, 0.f, 0.f});
        d = mfma(a1, bo[1][ct], d);
        *(LAS float*)(L + L_RED + w * 1024 + g * 256 + (16 * ct + n) * 4) = sel4(d, g);
      }
    }
    bar();
    STAMP(22);
    if (tid < 64) {
      f32x4 s = {0.f, 0.f, 0.f, 0.f};
#pragma unroll
      for (int ww = 0; ww < 8; ++ww) s += *(LAS f32x4*)(L + L_RED + ww * 1024 + tid * 16);
      ST_SC1((v4u{nosent(s.x), nosent(s.y), nosent(s.z), nosent(s.w)}), WS_ATT + ((X * 4 + g) * 512 + 64 * (J - J_B) + 4 * n) * 4);
    }
    STAMP(13);
    return;
  }
  const int pc = 7 * X + (J - J_C);
  {
    const int r0 = 64 * w + (lane >> 3), ch = img_chunk(lane, r0);
    const GAS unsigned char* s = (const GAS unsigned char*)((ch >> 2) ? p.wu : p.wg) + ((size_t)r0 * 1792 + 32 * pc + 8 * (ch & 3)) * 2;
#pragma unroll
    for (int e = 0; e < 8; ++e) glds16((const void*)(s + (size_t)e * 8 * 1792 * 2), lds0 + L_W + w * 8192 + e * 1024);
    const int rd = lane >> 3;
    const GAS unsigned char* sd = (const GAS unsigned char*)p.wd + ((size_t)(32 * pc + rd) * 512 + 64 * w + 8 * img_chunk(lane, rd)) * 2;
#pragma unroll
    for (int e = 0; e < 4; ++e) glds16((const void*)(sd + (size_t)e * 8 * 512 * 2), lds0 + L_WD + w * 4096 + e * 1024);
  }
  const v2u xr = *(const GAS v2u*)((const GAS unsigned char*)p.x + (tb * 512 + tk) * 2);
  STAMP(12); STAMP(13);
  {
    v4u at[8];
    for (unsigned spin = 0; spin < SPIN_MAX; ++spin) {
      unsigned m = 0;
#pragma unroll
      for (int i = 0; i < 8; ++i) { at[i] = LD_SC1(WS_ATT + ((i * 4 + tb) * 512 + tk) * 4); m = max(m, umax4(at[i])); }
      if (all_lanes(m != SENT)) break;
      asm volatile("" ::: "memory");
    }
    STAMP(14);
    asm volatile("s_waitcnt vmcnt(0)" ::: "memory");
    v4u bg[2][4], bd[4];
#pragma unroll
    for (int ct = 0; ct < 4; ++ct) { lptr wp = L + L_W + w * 8192 + frag_base(lane, ct); bg[0][ct] = w_frag(wp, 0); bg[1][ct] = w_frag(wp, 1); }
    {
      const int q = (lane >> 2) & 3;
#pragma unroll
      for (int kp = 0; kp < 4; ++kp) {
        const int ra = 8 * kp + q, rb_ = ra + 4;
        bd[kp] = tr_frag(L + L_WD + w * 4096 + ra * 128 + ((g ^ ((ra >> 1) & 3)) * 32) + (lane & 3) * 8, L + L_WD + w * 4096 + rb_ * 128 + ((g ^ ((rb_ >> 1) & 3)) * 32) + (lane & 3) * 8);
      }
    }
    STAMP(19);
    const v2u ga = *(LAS v2u*)(L + L_GATE + tb * 4096 + tk * 2), sb = *(LAS v2u*)(L + L_GATE + tb * 4096 + 1024 + tk * 2), cb = *(LAS v2u*)(L + L_GATE + tb * 4096 + 2048 + tk * 2);
    const float gav[4] = {bf_lo(ga.x), bf_hi(ga.x), bf_lo(ga.y), bf_hi(ga.y)}, sbv[4] = {bf_lo(sb.x), bf_hi(sb.x), bf_lo(sb.y), bf_hi(sb.y)},
                cbv[4] = {bf_lo(cb.x), bf_hi(cb.x), bf_lo(cb.y), bf_hi(cb.y)}, xs[4] = {bf_lo(xr.x), bf_hi(xr.x), bf_lo(xr.y), bf_hi(xr.y)};
    float x2[4];
#pragma unroll
    for (int e = 0; e < 4; ++e) {
      float a = 0.f;
#pragma unroll
      for (int i = 0; i < 8; ++i) a += __uint_as_float(at[i][e]);
      x2[e] = rb(xs[e] + rb(gav[e] * rb(a)));
    }
    *(LAS v2u*)(L + L_X + (tb * 512 + tk) * 2) = v2u{pk2(x2[0], x2[1]), pk2(x2[2], x2[3])};
    float sum, m2;
    ln_wave_stats(x2, sum, m2);
    if (lane == 0) *(LAS v2u*)(L + M_LN + w * 8) = v2u{__float_as_uint(sum), __float_as_uint(m2)};
    bar();
    STAMP(20);
    float mean_r, den_r;
    ln_row(L, tb, mean_r, den_r);
    float h2[4];
#pragma unroll
    for (int e = 0; e < 4; ++e) h2[e] = ln_mod(x2[e], mean_r, den_r, cbv[e], sbv[e]);
    *(LAS v2u*)(L + L_ACT + tb * RS + act_off(tk)) = v2u{pk2(h2[0], h2[1]), pk2(h2[2], h2[3])};
    bar();
    STAMP(21);
    {
      lptr ap = L + L_ACT + (lane & 3) * RS + g * 16 + (2 * w) * 64;
      const v4u a0 = lds128(ap), a1 = lds128(ap + 64);
#pragma unroll
      for (int ct = 0; ct < 4; ++ct) {
        f32x4 d = mfma(a0, bg[0][ct], f32x4{0.f, 0.f, 0.f, 0.f});
        d = mfma(a1, bg[1][ct], d);
        *(LAS float*)(L + L_RED + w * 1024 + g * 256 + (16 * ct + n) * 4) = sel4(d, g);
      }
    }
    bar();
    STAMP(22);
    if (w < 2) {
      const int b = 2 * w + (lane >> 5), un = lane & 31;
      float s = 0.f, up = 0.f;
#pragma unroll
      for (int ww = 0; ww < 8; ++ww) { s += *(LAS float*)(L + L_RED + ww * 1024 + b * 256 + un * 4); up += *(LAS float*)(L + L_RED + ww * 1024 + b * 256 + 128 + un * 4); }
      s = rb(s); up = rb(up);
      const float u = 0.7978845608028654f * (s + 0.044715f * s * s * s);
      const float ge = rb(s / (1.f + __builtin_amdgcn_exp2f(-2.f * LOG2E * u)));
      *(LAS unsigned short*)(L + M_HID + b * 64 + un * 2) = (unsigned short)(__float_as_uint(rb(ge * up)) >> 16);
    }
    bar();
    STAMP(23);
    f32x4 d = {0.f, 0.f, 0.f, 0.f};
#pragma unroll
    for (int kp = 0; kp < 4; ++kp) {
      v4u a4 = lds128(L + M_HID + (n & 3) * 64 + kp * 16);
      if ((n >> 2) != g) a4 = v4u{0u, 0u, 0u, 0u};
      d = mfma(a4, bd[kp], d);
    }
    ST_SC1((v4u{nosent(d[0]), nosent(d[1]), nosent(d[2]), nosent(d[3])}), WS_DOWN + pc * 8192 + (64 * w + lane) * 16);
  }
  STAMP(15);
  if (J < J_F_END) {
    const int f = 4 * X + (J - J_C);
    if (w < 7) {
      v4u dv[2];
      for (unsigned spin = 0; spin < SPIN_MAX; ++spin) {
#pragma unroll
        for (int i = 0; i < 2; ++i) dv[i] = LD_SC1(WS_DOWN + (28 * i + 4 * w + g) * 8192 + (16 * f + n) * 16);
        if (all_lanes(max(umax4(dv[0]), umax4(dv[1])) != SENT)) break;
        asm volatile("" ::: "memory");
      }
      f32x4 s;
#pragma unroll
      for (int b = 0; b < 4; ++b) s[b] = x32_sum(x16_sum(__uint_as_float(dv[0][b]) + __uint_as_float(dv[1][b])));
      if (lane < 16) *(LAS f32x4*)(L + L_RED + w * 256 + lane * 16) = s;
    }
    bar();
    if (tid < 64) {
      const int b = tid >> 4, col = 16 * f + n;
      float s = 0.f;
#pragma unroll
      for (int ww = 0; ww < 7; ++ww) s += *(LAS float*)(L + L_RED + ww * 256 + n * 16 + b * 4);
      const float y = rb(s);
      const float gb = bf_lo(*(LAS unsigned short*)(L + L_GATE + b * 4096 + 3072 + col * 2));
      const float x2 = bf_lo(*(LAS unsigned short*)(L + L_X + (b * 512 + col) * 2));
      const unsigned ob = pk2(x2 + rb(gb * y), 0.f) & 0xffffu;
      const unsigned o2 = ob | (dppu<XOR1>(ob) << 16);
      if ((n & 1) == 0) *(GAS unsigned*)((GAS unsigned char*)p.out + (b * 512 + col) * 2) = o2;
    }
  }
#undef tid
#undef lane
#undef g
#undef n
}
}

extern "C" void kernel_launch(void* const* d_in, const int* in_sizes, int n_in, void* d_out, int out_size, void* d_ws, size_t ws_size, hipStream_t stream) {
  (void)in_sizes; (void)n_in; (void)out_size; (void)ws_size;
  Params p;
  p.x = d_in[0]; p.k = d_in[1]; p.v = d_in[2]; p.wq = d_in[3]; p.wo = d_in[4];
  p.wg = d_in[6]; p.wu = d_in[7]; p.wd = d_in[8]; p.c = d_in[9]; p.wm = d_in[10];
  p.out = d_out; p.ws = d_ws;
  hipLaunchKernelGGL(adaln_block_kernel, dim3(NCU), dim3(NTHR), 0, stream, p);
}
```

```cpp
#include <hip/hip_runtime.h>
#include <hip/hip_bf16.h>
#include <stdint.h>

#define GAS __attribute__((address_space(1)))
#define LAS __attribute__((address_space(3)))
typedef unsigned v4u __attribute__((ext_vector_type(4)));
typedef unsigned v2u __attribute__((ext_vector_type(2)));
typedef float f32x4 __attribute__((ext_vector_type(4)));
typedef short s16x4 __attribute__((ext_vector_type(4)));
typedef __bf16 bf16x8 __attribute__((ext_vector_type(8)));
typedef __bf16 bf16x2 __attribute__((ext_vector_type(2)));
typedef LAS unsigned char* lptr;

#define PACE 4
#define FIRST 1
#define PACE_H PACE
#define LATE_WD 0
#define W_AT 3
#define EARLY_V2 0
#define STAMPS 0

namespace {
constexpr int NCU = 256, NTHR = 512;
constexpr unsigned KV_BYTES = 4u * 8u * 8192u * 128u * 2u;
constexpr unsigned WS_EPOCH = 0, WS_READY = 4096, WS_H = 8192, WS_MP = 16384,
                   WS_QP = WS_MP + 48 * 4 * 2048,
                   WS_GATE = WS_QP + 8 * 4 * 4 * 2048,
                   WS_STAT = WS_GATE + 16384, WS_ATT = WS_STAT + 8192,
                   WS_PART = WS_ATT + 65536, WS_DOWN = WS_PART + 256 * 2048, WS_END = WS_DOWN + 56 * 8192, WS_STAMP = WS_END + 65536;
constexpr int RS = 1088;
constexpr int L_WM = 0, L_WQ = 16384, L_WD = 0, L_W = 32768, L_MRG = 98304, L_GATE = L_MRG + 17408, L_ACT = L_GATE + 16384, L_ACTH = L_ACT + 4352, L_X = L_ACTH + 4352,
              L_Q = L_X + 4096, L_RED = L_Q + 1024, L_MISC = L_RED + 8192, L_BYTES = L_MISC + 1024;
constexpr int MRG_H = 528, MRG_W = 4 * MRG_H;
constexpr int M_LN = L_MISC, M_MX = L_MISC + 128, M_LS = L_MISC + 256, M_HID = L_MISC + 384, M_FLAG = L_MISC + 640, M_CLR = L_MISC + 644, M_CNT = L_MISC + 648, M_FW = L_MISC + 768;
constexpr unsigned SENT = 0xFFFFFFFFu;
constexpr unsigned SPIN_MAX = 1u << 15;
constexpr int AUX_NT = 2, AUX_SC1 = 16;
constexpr float LOG2E = 1.4426950408889634f;
constexpr int J_G = 8, J_H = 24, J_GR_END = 28;
constexpr int J_B = 8, J_C = 16, J_F_END = 20, J_C_END = 23;

struct Params { const void* x; const void* k; const void* v; const void* wq; const void* wo; const void* wg; const void* wu; const void* wd; const void* c; const void* wm; void* out; void* ws; };

__device__ __forceinline__ float bf_lo(unsigned u) { return __builtin_bit_cast(float, u << 16); }
__device__ __forceinline__ float bf_hi(unsigned u) { return __builtin_bit_cast(float, u & 0xffff0000u); }
__device__ __forceinline__ unsigned pk2(float a, float b) { bf16x2 r = {(__bf16)a, (__bf16)b}; return __builtin_bit_cast(unsigned, r); }
__device__ __forceinline__ float rb(float a) { return bf_lo(pk2(a, a)); }
template <int CTRL> __device__ __forceinline__ float dpp(float x) { return __builtin_bit_cast(float, __builtin_amdgcn_mov_dpp(__builtin_bit_cast(int, x), CTRL, 0xf, 0xf, true)); }
template <int CTRL> __device__ __forceinline__ unsigned dppu(unsigned x) { return (unsigned)__builtin_amdgcn_mov_dpp((int)x, CTRL, 0xf, 0xf, true); }
constexpr int XOR1 = 0xB1, XOR2 = 0x4E, HMIR = 0x141, ROR4 = 0x124, ROR8 = 0x128;
__device__ __forceinline__ float x16_sum(float x) { auto s = __builtin_amdgcn_permlane16_swap(__float_as_uint(x), __float_as_uint(x), false, false); return __uint_as_float(s[0]) + __uint_as_float(s[1]); }
__device__ __forceinline__ float x32_sum(float x) { auto s = __builtin_amdgcn_permlane32_swap(__float_as_uint(x), __float_as_uint(x), false, false); return __uint_as_float(s[0]) + __uint_as_float(s[1]); }
__device__ __forceinline__ float x16_max(float x) { auto s = __builtin_amdgcn_permlane16_swap(__float_as_uint(x), __float_as_uint(x), false, false); return fmaxf(__uint_as_float(s[0]), __uint_as_float(s[1])); }
__device__ __forceinline__ float x32_max(float x) { auto s = __builtin_amdgcn_permlane32_swap(__float_as_uint(x), __float_as_uint(x), false, false); return fmaxf(__uint_as_float(s[0]), __uint_as_float(s[1])); }
__device__ __forceinline__ float row_sum(float x) { x += dpp<XOR1>(x); x += dpp<XOR2>(x); x += dpp<HMIR>(x); x += dpp<ROR8>(x); return x; }
__device__ __forceinline__ float wave_sum(float x) { return x32_sum(x16_sum(row_sum(x))); }
__device__ __forceinline__ f32x4 mfma(v4u a, v4u b, f32x4 c) { return __builtin_amdgcn_mfma_f32_16x16x32_bf16(__builtin_bit_cast(bf16x8, a), __builtin_bit_cast(bf16x8, b), c, 0, 0, 0); }
__device__ __forceinline__ v2u tr_read(lptr p) { s16x4 r = __builtin_amdgcn_ds_read_tr16_b64_v4i16((LAS s16x4*)p); return __builtin_bit_cast(v2u, r); }
__device__ __forceinline__ v4u tr_frag(lptr p0, lptr p1) { v2u a = tr_read(p0), b = tr_read(p1); return v4u{a.x, a.y, b.x, b.y}; }
__device__ __forceinline__ v4u lds128(lptr p) { return *(LAS v4u*)p; }
__device__ __forceinline__ void bar() { asm volatile("s_waitcnt lgkmcnt(0)\n\ts_barrier" ::: "memory"); }
__device__ __forceinline__ void glds16(const void* gsrc, unsigned lds_dst) {
  unsigned keep;
  lds_dst = __builtin_amdgcn_readfirstlane(lds_dst);
  asm volatile("s_mov_b32 %0, m0\n\ts_mov_b32 m0, %2\n\ts_nop 0\n\tglobal_load_lds_dwordx4 %1, off nt\n\ts_mov_b32 m0, %0" : "=&s"(keep) : "v"(gsrc), "s"(lds_dst) : "memory");
}
__device__ __forceinline__ unsigned nosent(float f) { return min(__float_as_uint(f), SENT - 1u); }
__device__ __forceinline__ unsigned nosentu(unsigned u) { return min(u, SENT - 1u); }
__device__ __forceinline__ unsigned umax4(v4u a) { return max(max(a.x, a.y), max(a.z, a.w)); }
__device__ __forceinline__ bool all_lanes(bool ok) { return __builtin_amdgcn_ballot_w64(!ok) == 0ull; }
__device__ __forceinline__ float sel4(f32x4 d, int g) { return g == 0 ? d[0] : g == 1 ? d[1] : g == 2 ? d[2] : d[3]; }
__device__ __forceinline__ float fast_rcp(float x) { return __builtin_amdgcn_rcpf(x); }
#define RSRC(p, n) __builtin_amdgcn_make_buffer_rsrc((void*)(p), 0, (int)(n), 0x00020000)
#define LD_SC1(off) __builtin_amdgcn_raw_buffer_load_b128(rws, (off), 0, AUX_SC1)
#define ST_SC1(v, off) __builtin_amdgcn_raw_buffer_store_b128((v), rws, (off), 0, AUX_SC1)
#define FENCE() __builtin_amdgcn_sched_barrier(0)
#define STAMP(i) do { } while (0)
#define DUMP() do { } while (0)

__device__ __forceinline__ int img_chunk(int lane, int row) { return ((((lane & 7) >> 1) ^ ((row >> 1) & 3)) << 1) | (lane & 1); }
__device__ __forceinline__ int frag_base(int lane, int ct) { const int g = lane >> 4, q = (lane >> 2) & 3; return (4 * g + q) * 128 + ((ct ^ ((2 * g + (q >> 1)) & 3)) * 32) + (lane & 3) * 8; }
__device__ __forceinline__ v4u w_frag(lptr base, int s) { return tr_frag(base + s * 4096, base + s * 4096 + 2048); }
__device__ __forceinline__ int act_off(int k) { return (k >> 5) * 64 + ((k >> 2) & 3) * 16 + ((k >> 4) & 1) * 8; }
__device__ __forceinline__ void ln_consts(float sum, float sumsq, float& mean_r, float& rden) {
  const float mean = sum * (1.f / 512.f);
  const float var = fmaxf(sumsq * (1.f / 512.f) - mean * mean, 0.f);
  mean_r = rb(mean);
  rden = fast_rcp(rb(__builtin_amdgcn_sqrtf(rb(rb(var) + 1e-5f))));
}
__device__ __forceinline__ float ln_mod(float x, float mean_r, float rden, float c1, float ss) {
  return rb(rb(rb(rb(x - mean_r) * rden) * c1) + ss);
}

__global__ void __launch_bounds__(NTHR, 2) adaln_block_kernel(Params p) {
  __shared__ __attribute__((aligned(16))) unsigned char smem[L_BYTES + (STAMPS ? 1024 : 0)];
  lptr L = (lptr)smem;
  const unsigned lds0 = (unsigned)(uintptr_t)L;
  const int tid = threadIdx.x, lane = tid & 63, w = __builtin_amdgcn_readfirstlane(tid >> 6);
  const int cu = blockIdx.x, X = cu & 7, J = cu >> 3;
  const int g = lane >> 4, n = lane & 15;
  const auto rws = RSRC(p.ws, WS_END);
  const auto rk = RSRC(p.k, KV_BYTES);
  const auto rv = RSRC(p.v, KV_BYTES);
  const bool roleM = J < J_H, roleH = J >= J_H, roleGR = J >= J_H && J < J_GR_END, roleB = J >= J_B && J < J_C, roleC = J >= J_C && J < J_C_END;
  const int kq = J & 3;
  STAMP(0);
  const int mg = J < J_G ? 2 * X + (J >> 2) : 16 + 4 * X + ((J - J_G) >> 2);
  {
    const int r0 = 16 * w + (lane >> 3), ch = img_chunk(lane, r0);
    if (roleM) {
      const GAS unsigned char* s = (const GAS unsigned char*)p.wm + ((size_t)(128 * kq + r0) * 3072 + 64 * mg + 8 * ch) * 2;
      glds16((const void*)s, lds0 + L_WM + w * 2048);
      glds16((const void*)(s + 8 * 3072 * 2), lds0 + L_WM + w * 2048 + 1024);
    }
    const GAS unsigned char* s = (const GAS unsigned char*)p.wq + ((size_t)(128 * kq + r0) * 4096 + 64 * (8 * X + (J >> 2)) + 8 * ch) * 2;
    glds16((const void*)s, lds0 + L_WQ + w * 2048);
    glds16((const void*)(s + 8 * 4096 * 2), lds0 + L_WQ + w * 2048 + 1024);
  }
  const unsigned long long ep = __hip_atomic_load((GAS unsigned long long*)p.ws, __ATOMIC_RELAXED, __HIP_MEMORY_SCOPE_AGENT);
  v2u cv = {0u, 0u};
  if (roleM && tid < 128) cv = *(const GAS v2u*)((const GAS unsigned char*)p.c + ((tid >> 5) * 512 + 128 * kq + 4 * (tid & 31)) * 2);
  asm volatile("s_barrier" ::: "memory");
  if (w == 1) {
    const v4u sent = {SENT, SENT, SENT, SENT};
    ST_SC1(sent, WS_QP + ((X * 4 + g) * 4 + kq) * 2048 + (J >> 2) * 256 + n * 16);
    ST_SC1(sent, WS_PART + cu * 2048 + lane * 16);
    ST_SC1(sent, WS_PART + cu * 2048 + 1024 + lane * 16);
    if (lane < 2) ST_SC1(sent, WS_STAT + cu * 32 + lane * 16);
    if (roleB) ST_SC1(sent, WS_ATT + ((X * 4 + g) * 512 + 64 * (J - J_B) + 4 * n) * 4);
    if (roleGR && n < 8) ST_SC1(sent, WS_GATE + g * 4096 + (4 * X + (J - J_H)) * 128 + n * 16);
    if (roleC) {
#pragma unroll
      for (int e = 0; e < 8; ++e) ST_SC1(sent, WS_DOWN + (7 * X + (J - J_C)) * 8192 + e * 1024 + lane * 16);
    }
  }
  FENCE();
  const bool roleT = roleB || roleC;
  const unsigned kvbase = (((unsigned)(J >> 3) * 8u + (unsigned)X) * 8192u + (unsigned)(J & 7) * 1024u) * 256u + (unsigned)w * 4096u;
  const unsigned koff = (4 * (lane & 3) + ((lane >> 2) & 1)) * 256 + (lane >> 3) * 16;
  v4u kf[8][4];
#define K_ISSUE(t) do { unsigned ko_ = koff; asm volatile("" : "+v"(ko_)); _Pragma("unroll") for (int q_ = 0; q_ < 4; ++q_) kf[t][q_] = __builtin_amdgcn_raw_buffer_load_b128(rk, ko_ + (q_ >> 1) * 512 + (q_ & 1) * 128, kvbase + (t) * 32768, AUX_NT); FENCE(); } while (0)
  K_ISSUE(0);
  if (FIRST > 1) K_ISSUE(1);
  if (tid == 0) { *(LAS unsigned*)(L + M_FLAG) = 0u; *(LAS unsigned*)(L + M_CLR) = 0u; *(LAS unsigned*)(L + M_CNT) = 0u; }
  const unsigned long long E = ep + 1ull;
  const unsigned ta = __builtin_amdgcn_readfirstlane((unsigned)E), tb2 = __builtin_amdgcn_readfirstlane(((unsigned)E * 0x9E3779B1u) ^ (unsigned)(E >> 32));
  if (roleM && tid < 128) {
    float cs[4] = {bf_lo(cv.x), bf_hi(cv.x), bf_lo(cv.y), bf_hi(cv.y)};
#pragma unroll
    for (int e = 0; e < 4; ++e) cs[e] = cs[e] / (1.f + expf(-cs[e]));
    *(LAS v2u*)(L + L_ACT + (tid >> 5) * RS + act_off(4 * (tid & 31))) = v2u{pk2(cs[0], cs[1]), pk2(cs[2], cs[3])};
  }
  STAMP(17);
  asm volatile("s_waitcnt vmcnt(%0)" ::"i"(4 * FIRST) : "memory");
  STAMP(18);
  if (w == 1) {
    if (lane == 0) ST_SC1((v4u{0u, ta, 0u, tb2}), WS_READY + cu * 16);
  }
  bar();
  if (w == 1 && lane == 0) *(volatile LAS unsigned*)(L + M_CLR) = 1u;
  STAMP(1);
  if (roleM && w < 4) {
    lptr ap = L + L_ACT + (lane & 3) * RS + g * 16;
    lptr wp = L + L_WM + frag_base(lane, w);
    f32x4 a0 = mfma(lds128(ap), w_frag(wp, 0), f32x4{0.f, 0.f, 0.f, 0.f}), a1 = mfma(lds128(ap + 64), w_frag(wp, 1), f32x4{0.f, 0.f, 0.f, 0.f});
    a0 = mfma(lds128(ap + 128), w_frag(wp, 2), a0); a1 = mfma(lds128(ap + 192), w_frag(wp, 3), a1);
    const float v = sel4(a0 + a1, g);
    const float vn = dpp<XOR1>(v);
    if ((n & 1) == 0) ST_SC1((v4u{__float_as_uint(v), ta, __float_as_uint(vn), tb2}), WS_MP + ((mg * 4 + kq) * 4 + g) * 512 + (8 * w + (n >> 1)) * 16);
    FENCE();
  }
  if (roleH && w == 0) {
    __builtin_amdgcn_s_setprio(2);
    const int eta = 8 * X + (J - J_H), b = (lane >> 2) & 3, pr = lane & 3;
    v4u xq[4];
#pragma unroll
    for (int i = 0; i < 4; ++i) xq[i] = *(const GAS v4u*)((const GAS unsigned char*)p.x + (g * 512 + 32 * n + 8 * i) * 2);
    const unsigned xp = *(const GAS unsigned*)((const GAS unsigned char*)p.x + (b * 512 + 8 * eta + 2 * pr) * 2);
    float s1 = 0.f, s2 = 0.f;
#pragma unroll
    for (int i = 0; i < 4; ++i)
#pragma unroll
      for (int e = 0; e < 4; ++e) { const float a = bf_lo(xq[i][e]), c = bf_hi(xq[i][e]); s1 += a + c; s2 += a * a + c * c; }
    s1 = row_sum(s1); s2 = row_sum(s2);
    float mean_r, rden;
    ln_consts(s1, s2, mean_r, rden);
    mean_r = __builtin_bit_cast(float, __builtin_amdgcn_ds_bpermute(64 * b, __builtin_bit_cast(int, mean_r)));
    rden = __builtin_bit_cast(float, __builtin_amdgcn_ds_bpermute(64 * b, __builtin_bit_cast(int, rden)));
    v4u ms, mc;
    for (unsigned spin = 0; spin < SPIN_MAX; ++spin) {
      ms = LD_SC1(WS_MP + (((eta >> 3) * 4 + g) * 4 + b) * 512 + ((eta & 7) * 4 + pr) * 16);
      mc = LD_SC1(WS_MP + (((8 + (eta >> 3)) * 4 + g) * 4 + b) * 512 + ((eta & 7) * 4 + pr) * 16);
      if (all_lanes((ms.y == ta) & (ms.w == tb2) & (mc.y == ta) & (mc.w == tb2))) break;
      asm volatile("" ::: "memory");
    }
    const float sa0 = rb(x32_sum(x16_sum(__uint_as_float(ms.x)))), sa1 = rb(x32_sum(x16_sum(__uint_as_float(ms.z))));
    const float ca0 = rb(x32_sum(x16_sum(__uint_as_float(mc.x)))), ca1 = rb(x32_sum(x16_sum(__uint_as_float(mc.z))));
    const unsigned hp = pk2(ln_mod(bf_lo(xp), mean_r, rden, rb(1.f + ca0), sa0), ln_mod(bf_hi(xp), mean_r, rden, rb(1.f + ca1), sa1));
    const unsigned hn = dppu<XOR1>(hp);
    if (lane < 16 && (pr & 1) == 0) ST_SC1((v4u{hp, ta, hn, tb2}), WS_H + eta * 128 + (b * 2 + (pr >> 1)) * 16);
    __builtin_amdgcn_s_setprio(0);
    FENCE();
  }
  STAMP(2);
#define PACE_WAITN(x) do { if (w != 0 && PACE >= 0) asm volatile("s_waitcnt vmcnt(%0)" ::"i"(PACE < 0 ? 0 : PACE + (x)) : "memory"); } while (0)
#define PACE_WAIT2() PACE_WAITN(4)
#define PACE_WAIT() PACE_WAITN(0)
  if (FIRST < 2) K_ISSUE(1);

  PACE_WAIT(); K_ISSUE(2); PACE_WAIT(); K_ISSUE(3);
  if (w == 0) {
    __builtin_amdgcn_s_setprio(2);
    v4u bq[4][4];
#pragma unroll
    for (int ct = 0; ct < 4; ++ct) { lptr wp = L + L_WQ + frag_base(lane, ct);
#pragma unroll
      for (int s = 0; s < 4; ++s) bq[ct][s] = w_frag(wp, s); }
    v4u hv[2];
    for (unsigned spin = 0; spin < SPIN_MAX; ++spin) {
      bool ok = true;
#pragma unroll
      for (int i = 0; i < 2; ++i) { hv[i] = LD_SC1(WS_H + (128 * kq + 64 * i + lane) * 16); ok &= (hv[i].y == ta) & (hv[i].w == tb2); }
#pragma unroll
      for (int i = 0; i < 4; ++i) { const v4u rd = LD_SC1(WS_READY + (64 * i + lane) * 16); ok &= (rd.y == ta) & (rd.w == tb2); }
      if (all_lanes(ok)) break;
      asm volatile("" ::: "memory");
    }
    STAMP(3);
#pragma unroll
    for (int i = 0; i < 2; ++i) { const int gm = 64 * i + lane; *(LAS v2u*)(L + L_ACTH + ((gm >> 1) & 3) * RS + act_off(8 * (gm >> 3) + 4 * (gm & 1))) = v2u{hv[i].x, hv[i].z}; }
    asm volatile("s_waitcnt lgkmcnt(0)" ::: "memory");
    lptr ap = L + L_ACTH + (lane & 3) * RS + g * 16;
    const v4u a0 = lds128(ap), a1 = lds128(ap + 64), a2 = lds128(ap + 128), a3 = lds128(ap + 192);
    float qv[4];
#pragma unroll
    for (int ct = 0; ct < 4; ++ct) {
      const f32x4 z = {0.f, 0.f, 0.f, 0.f};
      qv[ct] = sel4(mfma(a1, bq[ct][1], mfma(a0, bq[ct][0], z)) + mfma(a3, bq[ct][3], mfma(a2, bq[ct][2], z)), g);
    }
    while (*(volatile LAS unsigned*)(L + M_CLR) == 0u) __builtin_amdgcn_s_sleep(1);
    ST_SC1((v4u{nosent(qv[0]), nosent(qv[1]), nosent(qv[2]), nosent(qv[3])}), WS_QP + ((X * 4 + g) * 4 + kq) * 2048 + (J >> 2) * 256 + n * 16);
    if (cu == 0 && lane == 0) __hip_atomic_store((GAS unsigned long long*)p.ws, E, __ATOMIC_RELAXED, __HIP_MEMORY_SCOPE_AGENT);
    __builtin_amdgcn_s_setprio(0);
    FENCE();
    STAMP(4);
  }
  PACE_WAIT(); K_ISSUE(4); PACE_WAIT(); K_ISSUE(5);
  PACE_WAIT(); K_ISSUE(6); PACE_WAIT(); K_ISSUE(7);
  if (w == 0) {
    __builtin_amdgcn_s_setprio(2);
    v4u qp[8];
    for (unsigned spin = 0; spin < SPIN_MAX; ++spin) {
      unsigned m = 0;
#pragma unroll
      for (int i = 0; i < 8; ++i) { qp[i] = LD_SC1(WS_QP + ((X * 4 + (J >> 3)) * 4 + (i >> 1)) * 2048 + ((i & 1) * 64 + lane) * 16); m = max(m, umax4(qp[i])); }
      if (all_lanes(m != SENT)) break;
      asm volatile("" ::: "memory");
    }
#pragma unroll
    for (int hf = 0; hf < 2; ++hf)
#pragma unroll
      for (int ct = 0; ct < 4; ++ct) {
        const float s = (__uint_as_float(qp[hf][ct]) + __uint_as_float(qp[2 + hf][ct])) + (__uint_as_float(qp[4 + hf][ct]) + __uint_as_float(qp[6 + hf][ct]));
        *(LAS unsigned short*)(L + L_Q + (64 * (4 * hf + g) + 16 * ct + n) * 2) = (unsigned short)(pk2(s, s) & 0xffffu);
      }
    asm volatile("s_waitcnt lgkmcnt(0)" ::: "memory");
    STAMP(5);
    if (roleGR) {
      const int gg = 4 * X + (J - J_H);
      v4u gv[8];
      for (unsigned spin = 0; spin < SPIN_MAX; ++spin) {
        bool ok = true;
#pragma unroll
        for (int i = 0; i < 8; ++i) { gv[i] = LD_SC1(WS_MP + (((16 + gg) * 4 + (i >> 1)) * 4 + g) * 512 + ((i & 1) * 16 + n) * 16); ok &= (gv[i].y == ta) & (gv[i].w == tb2); }
        if (all_lanes(ok)) break;
        asm volatile("" ::: "memory");
      }
#pragma unroll
      for (int hf = 0; hf < 2; ++hf) {
        const float s0 = (__uint_as_float(gv[hf].x) + __uint_as_float(gv[2 + hf].x)) + (__uint_as_float(gv[4 + hf].x) + __uint_as_float(gv[6 + hf].x));
        const float s1 = (__uint_as_float(gv[hf].z) + __uint_as_float(gv[2 + hf].z)) + (__uint_as_float(gv[4 + hf].z) + __uint_as_float(gv[6 + hf].z));
        __builtin_amdgcn_raw_buffer_store_b32(nosentu(pk2(s0, s1)), rws, WS_GATE + g * 4096 + gg * 128 + (16 * hf + n) * 4, 0, AUX_SC1);
      }
    }
    __builtin_amdgcn_s_setprio(0);
    FENCE();
  }
  v4u vf[4][8];
#define V_ISSUE(u) do { unsigned vo_ = lane * 16; asm volatile("" : "+v"(vo_)); _Pragma("unroll") for (int j_ = 0; j_ < 8; ++j_) vf[u][j_] = __builtin_amdgcn_raw_buffer_load_b128(rv, vo_ + (j_ & 3) * 1024, kvbase + (2 * (u) + (j_ >> 2)) * 32768, AUX_NT); FENCE(); } while (0)
  PACE_WAIT(); V_ISSUE(0);
  PACE_WAIT2(); V_ISSUE(1);
  STAMP(6);
  bar();
  STAMP(7);
  const v4u qf0 = lds128(L + L_Q + (n & 3) * 256 + g * 32 + ((n >> 2) & 1) * 16), qf1 = lds128(L + L_Q + (n & 3) * 256 + 128 + g * 32 + ((n >> 2) & 1) * 16);
  float S[8][4];
#define QK(t) do { const f32x4 z_ = {0.f, 0.f, 0.f, 0.f}; \
    const f32x4 d0_ = mfma(kf[t][1], qf1, mfma(kf[t][0], qf0, z_)), d1_ = mfma(kf[t][3], qf1, mfma(kf[t][2], qf0, z_)); \
    _Pragma("unroll") for (int r_ = 0; r_ < 4; ++r_) { \
      auto s_ = __builtin_amdgcn_permlane32_swap(__float_as_uint(d0_[r_]), __float_as_uint(d1_[r_]), false, false); \
      S[t][r_] = __uint_as_float(s_[0]) + dpp<ROR4>(__uint_as_float(s_[1])); } } while (0)
  QK(0); QK(1); QK(2); QK(3); QK(4); QK(5); QK(6); QK(7); FENCE();
  STAMP(8);
  V_ISSUE(2); V_ISSUE(3);
  unsigned S2[8][2];
  float mx = -__builtin_inff();
#pragma unroll
  for (int t = 0; t < 8; ++t)
#pragma unroll
    for (int r = 0; r < 2; ++r) {
      const unsigned a = pk2(S[t][2 * r], S[t][2 * r + 1]);
      S2[t][r] = pk2(bf_lo(a) * 0.08838834764831845f, bf_hi(a) * 0.08838834764831845f);
      mx = fmaxf(mx, fmaxf(bf_lo(S2[t][r]), bf_hi(S2[t][r])));
    }
  FENCE();
  FENCE();
  mx = x32_max(x16_max(mx));
  float ls = 0.f;
  v4u pf[4];
#pragma unroll
  for (int u = 0; u < 4; ++u) {
    float e[8];
#pragma unroll
    for (int j = 0; j < 8; ++j) { const unsigned sp = S2[2 * u + (j >> 2)][(j & 3) >> 1]; e[j] = __builtin_amdgcn_exp2f((((j & 1) ? bf_hi(sp) : bf_lo(sp)) - mx) * LOG2E); ls += e[j]; }
    pf[u] = v4u{pk2(e[0], e[1]), pk2(e[2], e[3]), pk2(e[4], e[5]), pk2(e[6], e[7])};
  }
  ls = x32_sum(x16_sum(ls));
  if (lane < 4) { *(LAS float*)(L + M_MX + lane * 32 + w * 4) = mx; *(LAS float*)(L + M_LS + lane * 32 + w * 4) = ls; }
  {
    asm volatile("s_waitcnt lgkmcnt(0)" ::: "memory");
    unsigned old = 0;
    if (lane == 0) old = __hip_atomic_fetch_add((LAS unsigned*)(L + M_CNT), 1u, __ATOMIC_RELAXED, __HIP_MEMORY_SCOPE_WORKGROUP);
    if (__builtin_amdgcn_readfirstlane(old) == 7u) {
      const float mw = *(volatile LAS float*)(L + M_MX + ((lane >> 3) & 3) * 32 + (lane & 7) * 4), lw = *(volatile LAS float*)(L + M_LS + ((lane >> 3) & 3) * 32 + (lane & 7) * 4);
      float M = fmaxf(mw, dpp<XOR1>(mw)); M = fmaxf(M, dpp<XOR2>(M)); M = fmaxf(M, dpp<HMIR>(M));
      const float f = __builtin_amdgcn_exp2f((mw - M) * LOG2E);
      float Lc = f * lw; Lc += dpp<XOR1>(Lc); Lc += dpp<XOR2>(Lc); Lc += dpp<HMIR>(Lc);
      if (lane < 32) *(LAS float*)(L + M_FW + lane * 4) = f;
      if (lane < 32 && (lane & 7) == 0) __builtin_amdgcn_raw_buffer_store_b64((v2u{nosent(M), nosent(Lc)}), rws, WS_STAT + cu * 32 + (lane >> 3) * 8, 0, AUX_SC1);
    }
  }
  FENCE();
  STAMP(9);
  v4u wr[8];
#define W_LOADS() do { \
     \
    const int pc_ = 7 * X + (J - J_C); \
    const auto rwa = RSRC(roleB ? p.wo : p.wg, roleB ? 4096 * 512 * 2 : roleC ? 512 * 1792 * 2 : 0); \
    const auto rwb = RSRC(roleB ? p.wo : p.wu, roleB ? 4096 * 512 * 2 : roleC ? 512 * 1792 * 2 : 0); \
    const int rB = 64 * w + (lane >> 3); \
    const unsigned vo = roleB ? ((512 * X + rB) * 512 + 64 * (J - J_B) + 8 * img_chunk(lane, rB)) * 2 : ((64 * w + (lane >> 2)) * 1792 + 32 * pc_ + 8 * (lane & 3)) * 2; \
    const unsigned st = roleB ? 8 * 512 * 2 : 16 * 1792 * 2; \
_Pragma("unroll") \
    for (int e = 0; e < 4; ++e) wr[e] = __builtin_amdgcn_raw_buffer_load_b128(rwa, vo, e * st, AUX_NT); \
_Pragma("unroll") \
    for (int e = 0; e < 4; ++e) wr[4 + e] = __builtin_amdgcn_raw_buffer_load_b128(rwb, vo, (roleB ? 4 + e : e) * st, AUX_NT); \
  } while (0)
  f32x4 oa[8];
#pragma unroll
  for (int d = 0; d < 8; ++d) oa[d] = f32x4{0.f, 0.f, 0.f, 0.f};
#define PV(u) do { _Pragma("unroll") for (int d = 0; d < 8; ++d) { \
      const unsigned sel = (d & 1) ? 0x07060302u : 0x05040100u; v4u a; \
      _Pragma("unroll") for (int i = 0; i < 4; ++i) a[i] = __builtin_amdgcn_perm(vf[u][2 * i + 1][d >> 1], vf[u][2 * i][d >> 1], sel); \
      oa[d] = mfma(a, pf[u], oa[d]); } FENCE(); } while (0)
  PV(0); PV(1);
  if (W_AT == 1) W_LOADS();
  PV(2);
  if (W_AT == 2) W_LOADS();
  PV(3);
  if (W_AT == 3) { W_LOADS(); FENCE(); }
  STAMP(10);
  int tid2 = threadIdx.x; asm volatile("" : "+v"(tid2));
#define tid tid2
  const int lane2 = tid2 & 63;
#define lane lane2
  const int g2 = lane2 >> 4, n2 = lane2 & 15;
#define g g2
#define n n2
  const int tb = tid2 >> 7, tk = 4 * (tid2 & 127);
  if (n < 4) {
#pragma unroll
    for (int r = 0; r < 4; ++r) {
      *(LAS f32x4*)(L + L_MRG + w * MRG_W + n * MRG_H + r * 128 + g * 32) = f32x4{oa[0][r], oa[1][r], oa[2][r], oa[3][r]};
      *(LAS f32x4*)(L + L_MRG + w * MRG_W + n * MRG_H + r * 128 + g * 32 + 16) = f32x4{oa[4][r], oa[5][r], oa[6][r], oa[7][r]};
    }
  }
  bar();
  {
    const f32x4 f0 = *(LAS f32x4*)(L + M_FW + tb * 32), f1 = *(LAS f32x4*)(L + M_FW + tb * 32 + 16);
    float o = 0.f;
#pragma unroll
    for (int ww = 0; ww < 8; ++ww) o += (ww < 4 ? f0[ww & 3] : f1[ww & 3]) * *(LAS float*)(L + L_MRG + ww * MRG_W + tb * MRG_H + (tid & 127) * 4);
    __builtin_amdgcn_raw_buffer_store_b32(nosent(o), rws, WS_PART + cu * 2048 + tid * 4, 0, AUX_SC1);
  }
  STAMP(11);
  if (!(roleB || roleC)) { STAMP(16); DUMP(); return; }
  v4u wdr[4], gt[2];
  v2u xr;
  if (roleC) {
#pragma unroll
    for (int e = 0; e < 2; ++e) gt[e] = LD_SC1(WS_GATE + ((2 * w + e) * 64 + lane) * 16);
    xr = *(const GAS v2u*)((const GAS unsigned char*)p.x + tid * 8);
    const auto rd = RSRC(p.wd, 1792 * 512 * 2);
    const unsigned o = ((32 * (7 * X + (J - J_C)) + (lane >> 3)) * 512 + 64 * w + 8 * img_chunk(lane, lane >> 3)) * 2;
#pragma unroll
    for (int e = 0; e < 4; ++e) wdr[e] = __builtin_amdgcn_raw_buffer_load_b128(rd, o, e * 8 * 512 * 2, AUX_NT);
  }
  if (roleB) {
#pragma unroll
    for (int e = 0; e < 8; ++e) *(LAS v4u*)(L + L_W + w * 8192 + e * 1024 + lane * 16) = wr[e];
  } else {
    const int rw = lane >> 2, x = (rw >> 1) & 3, c = lane & 3;
    lptr pg = L + L_W + w * 8192 + rw * 128 + ((((c >> 1) ^ x) << 1) | (c & 1)) * 16, pu = L + L_W + w * 8192 + rw * 128 + ((((2 + (c >> 1)) ^ x) << 1) | (c & 1)) * 16;
#pragma unroll
    for (int e = 0; e < 4; ++e) { *(LAS v4u*)(pg + e * 2048) = wr[e]; *(LAS v4u*)(pu + e * 2048) = wr[4 + e]; }
  }
  if (roleB) {
    const int bb = w >> 1, hf = lane >> 5;
    v2u st[2];
    for (unsigned spin = 0; spin < SPIN_MAX; ++spin) {
#pragma unroll
      for (int hh = 0; hh < 2; ++hh) st[hh] = __builtin_amdgcn_raw_buffer_load_b64(rws, WS_STAT + ((8 * bb + 2 * (lane & 3) + hf) * 8 + X) * 32 + (2 * (w & 1) + hh) * 8, 0, AUX_SC1);
      if (all_lanes(max(max(st[0].x, st[0].y), max(st[1].x, st[1].y)) != SENT)) break;
      asm volatile("" ::: "memory");
    }
    float wt[2][4];
#pragma unroll
    for (int hh = 0; hh < 2; ++hh) {
      const float ms = __uint_as_float(st[hh].x);
      float M = fmaxf(ms, dpp<XOR1>(ms)); M = fmaxf(M, dpp<XOR2>(M)); M = x32_max(M);
      const float f = __builtin_amdgcn_exp2f((ms - M) * LOG2E);
      float Ls = f * __uint_as_float(st[hh].y); Ls += dpp<XOR1>(Ls); Ls += dpp<XOR2>(Ls); Ls = x32_sum(Ls);
      const float x = f * fast_rcp(Ls);
      wt[hh][0] = dpp<0x00>(x); wt[hh][1] = dpp<0x55>(x); wt[hh][2] = dpp<0xAA>(x); wt[hh][3] = dpp<0xFF>(x);
    }
    asm volatile("s_waitcnt lgkmcnt(0)" ::: "memory");
    v4u bo[2][4];
#pragma unroll
    for (int ct = 0; ct < 4; ++ct) { lptr wp = L + L_W + w * 8192 + frag_base(lane, ct); bo[0][ct] = w_frag(wp, 0); bo[1][ct] = w_frag(wp, 1); }
    STAMP(19);
    v4u po[2][4];
    for (unsigned spin = 0; spin < SPIN_MAX; ++spin) {
      unsigned m = 0;
#pragma unroll
      for (int hh = 0; hh < 2; ++hh)
#pragma unroll
        for (int i = 0; i < 4; ++i) { po[hh][i] = LD_SC1(WS_PART + ((8 * bb + 2 * i + hf) * 8 + X) * 2048 + (2 * (w & 1) + hh) * 512 + (lane & 31) * 16); m = max(m, umax4(po[hh][i])); }
      if (all_lanes(m != SENT)) break;
      asm volatile("" ::: "memory");
    }
    STAMP(12);
    {
      const int pi = 4 * (lane & 31), dim = 32 * ((pi >> 3) & 3) + 8 * (pi >> 5) + (pi & 7);
#pragma unroll
      for (int hh = 0; hh < 2; ++hh) {
        float o[4];
#pragma unroll
        for (int e = 0; e < 4; ++e)
          o[e] = x32_sum(wt[hh][0] * __uint_as_float(po[hh][0][e]) + wt[hh][1] * __uint_as_float(po[hh][1][e]) + wt[hh][2] * __uint_as_float(po[hh][2][e]) + wt[hh][3] * __uint_as_float(po[hh][3][e]));
        if (lane < 32) *(LAS v2u*)(L + L_ACT + bb * RS + act_off(128 * (2 * (w & 1) + hh) + dim)) = v2u{pk2(o[0], o[1]), pk2(o[2], o[3])};
      }
    }
    bar();
    {
      lptr ap = L + L_ACT + (lane & 3) * RS + g * 16 + (2 * w) * 64;
      const v4u a0 = lds128(ap), a1 = lds128(ap + 64);
#pragma unroll
      for (int ct = 0; ct < 4; ++ct) {
        f32x4 d = mfma(a0, bo[0][ct], f32x4{0.f, 0.f, 0.f, 0.f});
        d = mfma(a1, bo[1][ct], d);
        *(LAS float*)(L + L_RED + w * 1024 + g * 256 + (16 * ct + n) * 4) = sel4(d, g);
      }
    }
    bar();
    if (tid < 64) {
      f32x4 s = {0.f, 0.f, 0.f, 0.f};
#pragma unroll
      for (int ww = 0; ww < 8; ++ww) s += *(LAS f32x4*)(L + L_RED + ww * 1024 + tid * 16);
      ST_SC1((v4u{nosent(s.x), nosent(s.y), nosent(s.z), nosent(s.w)}), WS_ATT + ((X * 4 + g) * 512 + 64 * (J - J_B) + 4 * n) * 4);
    }
    STAMP(13); STAMP(16); DUMP();
    return;
  }
  const int pc = 7 * X + (J - J_C);
#pragma unroll
  for (int e = 0; e < 4; ++e) *(LAS v4u*)(L + L_WD + w * 4096 + e * 1024 + lane * 16) = wdr[e];
  if (!all_lanes(umax4(gt[0]) != SENT && umax4(gt[1]) != SENT)) {
    for (unsigned spin = 0; spin < SPIN_MAX; ++spin) {
      asm volatile("" ::: "memory");
#pragma unroll
      for (int e = 0; e < 2; ++e) gt[e] = LD_SC1(WS_GATE + ((2 * w + e) * 64 + lane) * 16);
      if (all_lanes(umax4(gt[0]) != SENT && umax4(gt[1]) != SENT)) break;
    }
  }
#pragma unroll
  for (int e = 0; e < 2; ++e) *(LAS v4u*)(L + L_GATE + ((2 * w + e) * 64 + lane) * 16) = gt[e];
  bar();
  {
    const v2u ga = *(LAS v2u*)(L + L_GATE + tb * 4096 + tk * 2), sb = *(LAS v2u*)(L + L_GATE + tb * 4096 + 1024 + tk * 2), cb = *(LAS v2u*)(L + L_GATE + tb * 4096 + 2048 + tk * 2);
    const v2u xw = xr;
    const float gav[4] = {bf_lo(ga.x), bf_hi(ga.x), bf_lo(ga.y), bf_hi(ga.y)}, sbv[4] = {bf_lo(sb.x), bf_hi(sb.x), bf_lo(sb.y), bf_hi(sb.y)},
                c1v[4] = {rb(1.f + bf_lo(cb.x)), rb(1.f + bf_hi(cb.x)), rb(1.f + bf_lo(cb.y)), rb(1.f + bf_hi(cb.y))}, xs[4] = {bf_lo(xw.x), bf_hi(xw.x), bf_lo(xw.y), bf_hi(xw.y)};
    v4u bg[2][4], bd[4];
#pragma unroll
    for (int ct = 0; ct < 4; ++ct) { lptr wp = L + L_W + w * 8192 + frag_base(lane, ct); bg[0][ct] = w_frag(wp, 0); bg[1][ct] = w_frag(wp, 1); }
    {
      const int q = (lane >> 2) & 3;
#pragma unroll
      for (int kp = 0; kp < 4; ++kp) {
        const int ra = 8 * kp + q, rb_ = ra + 4;
        bd[kp] = tr_frag(L + L_WD + w * 4096 + ra * 128 + ((g ^ ((ra >> 1) & 3)) * 32) + (lane & 3) * 8, L + L_WD + w * 4096 + rb_ * 128 + ((g ^ ((rb_ >> 1) & 3)) * 32) + (lane & 3) * 8);
      }
    }
    STAMP(19);
    v4u at[8];
    for (unsigned spin = 0; spin < SPIN_MAX; ++spin) {
      unsigned m = 0;
#pragma unroll
      for (int i = 0; i < 8; ++i) { at[i] = LD_SC1(WS_ATT + ((i * 4 + tb) * 512 + tk) * 4); m = max(m, umax4(at[i])); }
      if (all_lanes(m != SENT)) break;
      asm volatile("" ::: "memory");
    }
    STAMP(14);
    float x2[4], s1, s2;
#pragma unroll
    for (int e = 0; e < 4; ++e) {
      const float a = ((__uint_as_float(at[0][e]) + __uint_as_float(at[1][e])) + (__uint_as_float(at[2][e]) + __uint_as_float(at[3][e]))) +
                      ((__uint_as_float(at[4][e]) + __uint_as_float(at[5][e])) + (__uint_as_float(at[6][e]) + __uint_as_float(at[7][e])));
      x2[e] = rb(xs[e] + rb(gav[e] * rb(a)));
    }
    s1 = wave_sum((x2[0] + x2[1]) + (x2[2] + x2[3]));
    s2 = wave_sum((x2[0] * x2[0] + x2[1] * x2[1]) + (x2[2] * x2[2] + x2[3] * x2[3]));
    if (lane == 0) *(LAS v2u*)(L + M_LN + w * 8) = v2u{__float_as_uint(s1), __float_as_uint(s2)};
    *(LAS v2u*)(L + L_X + tid * 8) = v2u{pk2(x2[0], x2[1]), pk2(x2[2], x2[3])};
    bar();
    STAMP(20);
    {
      const f32x4 s = *(LAS f32x4*)(L + M_LN + tb * 16);
      float mean_r, rden;
      ln_consts(s.x + s.z, s.y + s.w, mean_r, rden);
      float h2[4];
#pragma unroll
      for (int e = 0; e < 4; ++e) h2[e] = ln_mod(x2[e], mean_r, rden, c1v[e], sbv[e]);
      *(LAS v2u*)(L + L_ACT + tb * RS + act_off(tk)) = v2u{pk2(h2[0], h2[1]), pk2(h2[2], h2[3])};
    }
    bar();
    STAMP(21);
    {
      lptr ap = L + L_ACT + (lane & 3) * RS + g * 16 + (2 * w) * 64;
      const v4u a0 = lds128(ap), a1 = lds128(ap + 64);
#pragma unroll
      for (int ct = 0; ct < 4; ++ct) {
        f32x4 d = mfma(a0, bg[0][ct], f32x4{0.f, 0.f, 0.f, 0.f});
        d = mfma(a1, bg[1][ct], d);
        *(LAS float*)(L + L_RED + w * 1024 + g * 256 + (16 * ct + n) * 4) = sel4(d, g);
      }
    }
    bar();
    STAMP(22);
    if (w < 2) {
      const int b = 2 * w + (lane >> 5), un = lane & 31;
      float s = 0.f, up = 0.f;
#pragma unroll
      for (int ww = 0; ww < 8; ++ww) { s += *(LAS float*)(L + L_RED + ww * 1024 + b * 256 + un * 4); up += *(LAS float*)(L + L_RED + ww * 1024 + b * 256 + 128 + un * 4); }
      s = rb(s); up = rb(up);
      const float u = 0.7978845608028654f * (s + 0.044715f * s * s * s);
      const float ge = rb(s * fast_rcp(1.f + __builtin_amdgcn_exp2f(-2.f * LOG2E * u)));
      *(LAS unsigned short*)(L + M_HID + b * 64 + un * 2) = (unsigned short)(__float_as_uint(rb(ge * up)) >> 16);
    }
    bar();
    STAMP(23);
    f32x4 d0 = {0.f, 0.f, 0.f, 0.f}, d1 = {0.f, 0.f, 0.f, 0.f};
#pragma unroll
    for (int kp = 0; kp < 4; kp += 2) {
      v4u a4 = lds128(L + M_HID + (n & 3) * 64 + kp * 16), a5 = lds128(L + M_HID + (n & 3) * 64 + kp * 16 + 16);
      if ((n >> 2) != g) { a4 = v4u{0u, 0u, 0u, 0u}; a5 = v4u{0u, 0u, 0u, 0u}; }
      d0 = mfma(a4, bd[kp], d0); d1 = mfma(a5, bd[kp + 1], d1);
    }
    const f32x4 d = d0 + d1;
    ST_SC1((v4u{nosent(d[0]), nosent(d[1]), nosent(d[2]), nosent(d[3])}), WS_DOWN + pc * 8192 + (64 * w + lane) * 16);
  }
  STAMP(15);
  if (J < J_F_END) {
    const int f = 4 * X + (J - J_C);
    float gb = 0.f, x2 = 0.f;
    if (tid < 64) {
      gb = bf_lo(*(LAS unsigned short*)(L + L_GATE + g * 4096 + 3072 + (16 * f + n) * 2));
      x2 = bf_lo(*(LAS unsigned short*)(L + L_X + (g * 512 + 16 * f + n) * 2));
    }
    if (w < 7) {
      v4u dv[2];
      for (unsigned spin = 0; spin < SPIN_MAX; ++spin) {
#pragma unroll
        for (int i = 0; i < 2; ++i) dv[i] = LD_SC1(WS_DOWN + (28 * i + 4 * w + g) * 8192 + (16 * f + n) * 16);
        if (all_lanes(max(umax4(dv[0]), umax4(dv[1])) != SENT)) break;
        asm volatile("" ::: "memory");
      }
      f32x4 s;
#pragma unroll
      for (int b = 0; b < 4; ++b) s[b] = x32_sum(x16_sum(__uint_as_float(dv[0][b]) + __uint_as_float(dv[1][b])));
      if (lane < 16) *(LAS f32x4*)(L + L_RED + w * 256 + lane * 16) = s;
    }
    bar();
    if (tid < 64) {
      float s = 0.f;
#pragma unroll
      for (int ww = 0; ww < 7; ++ww) s += *(LAS float*)(L + L_RED + ww * 256 + n * 16 + g * 4);
      const unsigned ob = pk2(x2 + rb(gb * rb(s)), 0.f) & 0xffffu;
      const unsigned o2 = ob | (dppu<XOR1>(ob) << 16);
      if ((n & 1) == 0) *(GAS unsigned*)((GAS unsigned char*)p.out + (g * 512 + 16 * f + n) * 2) = o2;
    }
  }
  STAMP(16); DUMP();
#undef tid
#undef lane
#undef g
#undef n
}
}

extern "C" void kernel_launch(void* const* d_in, const int* in_sizes, int n_in, void* d_out, int out_size, void* d_ws, size_t ws_size, hipStream_t stream) {
  (void)in_sizes; (void)n_in; (void)out_size; (void)ws_size;
  Params p;
  p.x = d_in[0]; p.k = d_in[1]; p.v = d_in[2]; p.wq = d_in[3]; p.wo = d_in[4];
  p.wg = d_in[6]; p.wu = d_in[7]; p.wd = d_in[8]; p.c = d_in[9]; p.wm = d_in[10];
  p.out = d_out; p.ws = d_ws;
  hipLaunchKernelGGL(adaln_block_kernel, dim3(NCU), dim3(NTHR), 0, stream, p);
}
```

```cpp
#include <hip/hip_runtime.h>
#include <hip/hip_bf16.h>
#include <stdint.h>

#define GAS __attribute__((address_space(1)))
#define LAS __attribute__((address_space(3)))
typedef unsigned v4u __attribute__((ext_vector_type(4)));
typedef unsigned v2u __attribute__((ext_vector_type(2)));
typedef float f32x4 __attribute__((ext_vector_type(4)));
typedef short s16x4 __attribute__((ext_vector_type(4)));
typedef __bf16 bf16x8 __attribute__((ext_vector_type(8)));
typedef __bf16 bf16x2 __attribute__((ext_vector_type(2)));
typedef LAS unsigned char* lptr;

#define PACE 5
#define FIRST 1
#define PACE_H PACE
#define LATE_WD 0
#define PA PACE
#define PB PACE
#define W_GATE 1
#define W_AT 3
#define EARLY_V2 0
#define STAMPS 0

namespace {
constexpr int NCU = 256, NTHR = 512;
constexpr unsigned KV_BYTES = 4u * 8u * 8192u * 128u * 2u;
constexpr unsigned WS_EPOCH = 0, WS_READY = 4096, WS_H = 8192, WS_MP = 16384,
                   WS_QP = WS_MP + 48 * 4 * 2048,
                   WS_GATE = WS_QP + 8 * 4 * 4 * 2048,
                   WS_STAT = WS_GATE + 16384, WS_ATT = WS_STAT + 8192,
                   WS_PART = WS_ATT + 8 * 32768,
                   WS_DOWN = WS_PART + 256 * 1024, WS_END = WS_DOWN + 56 * 8192, WS_STAMP = WS_END + 65536;
constexpr int RS = 1088;
constexpr int L_WM = 0, L_WQ = 16384, L_WD = 0, L_W = 32768, L_MRG = 98304, L_GATE = L_MRG + 18432, L_ACT = L_GATE + 16384, L_ACTH = L_ACT + 4352, L_X = L_ACTH + 4352,
              L_Q = L_X + 4096, L_RED = L_Q + 1024, L_MISC = L_RED + 8192, L_BYTES = L_MISC + 1024;
constexpr int MRG_H = 576, MRG_W = 4 * MRG_H;
constexpr int M_LN = L_MISC, M_MX = L_MISC + 128, M_LS = L_MISC + 256, M_HID = L_MISC + 384, M_FLAG = L_MISC + 640, M_CLR = L_MISC + 644, M_CNT = L_MISC + 648, M_CN2 = L_MISC + 652, M_FW = L_MISC + 768, M_ZERO = L_MISC + 896;
constexpr unsigned SENT = 0xFFFFFFFFu;
constexpr unsigned SPIN_MAX = 1u << 15;
constexpr int AUX_NT = 2, AUX_SC1 = 16;
constexpr float LOG2E = 1.4426950408889634f;
constexpr int J_G = 8, J_H = 24, J_GR_END = 28;
constexpr int J_B = 8, J_C = 16, J_F_END = 20, J_C_END = 23;

struct Params { const void* x; const void* k; const void* v; const void* wq; const void* wo; const void* wg; const void* wu; const void* wd; const void* c; const void* wm; void* out; void* ws; };

__device__ __forceinline__ float bf_lo(unsigned u) { return __builtin_bit_cast(float, u << 16); }
__device__ __forceinline__ float bf_hi(unsigned u) { return __builtin_bit_cast(float, u & 0xffff0000u); }
__device__ __forceinline__ unsigned pk2(float a, float b) { bf16x2 r = {(__bf16)a, (__bf16)b}; return __builtin_bit_cast(unsigned, r); }
__device__ __forceinline__ float rb(float a) { return bf_lo(pk2(a, a)); }
template <int CTRL> __device__ __forceinline__ float dpp(float x) { return __builtin_bit_cast(float, __builtin_amdgcn_mov_dpp(__builtin_bit_cast(int, x), CTRL, 0xf, 0xf, true)); }
template <int CTRL> __device__ __forceinline__ unsigned dppu(unsigned x) { return (unsigned)__builtin_amdgcn_mov_dpp((int)x, CTRL, 0xf, 0xf, true); }
constexpr int XOR1 = 0xB1, XOR2 = 0x4E, HMIR = 0x141, ROR4 = 0x124, ROR8 = 0x128;
__device__ __forceinline__ float x16_sum(float x) { auto s = __builtin_amdgcn_permlane16_swap(__float_as_uint(x), __float_as_uint(x), false, false); return __uint_as_float(s[0]) + __uint_as_float(s[1]); }
__device__ __forceinline__ float x32_sum(float x) { auto s = __builtin_amdgcn_permlane32_swap(__float_as_uint(x), __float_as_uint(x), false, false); return __uint_as_float(s[0]) + __uint_as_float(s[1]); }
__device__ __forceinline__ float x16_max(float x) { auto s = __builtin_amdgcn_permlane16_swap(__float_as_uint(x), __float_as_uint(x), false, false); return fmaxf(__uint_as_float(s[0]), __uint_as_float(s[1])); }
__device__ __forceinline__ float x32_max(float x) { auto s = __builtin_amdgcn_permlane32_swap(__float_as_uint(x), __float_as_uint(x), false, false); return fmaxf(__uint_as_float(s[0]), __uint_as_float(s[1])); }
__device__ __forceinline__ float row_sum(float x) { x += dpp<XOR1>(x); x += dpp<XOR2>(x); x += dpp<HMIR>(x); x += dpp<ROR8>(x); return x; }
__device__ __forceinline__ float wave_sum(float x) { return x32_sum(x16_sum(row_sum(x))); }
__device__ __forceinline__ void wave_sum2(float& x, float& y) {
  x += dpp<XOR1>(x); y += dpp<XOR1>(y); x += dpp<XOR2>(x); y += dpp<XOR2>(y); x += dpp<HMIR>(x); y += dpp<HMIR>(y); x += dpp<ROR8>(x); y += dpp<ROR8>(y);
  x = x16_sum(x); y = x16_sum(y); x = x32_sum(x); y = x32_sum(y);
}
__device__ __forceinline__ f32x4 mfma(v4u a, v4u b, f32x4 c) { return __builtin_amdgcn_mfma_f32_16x16x32_bf16(__builtin_bit_cast(bf16x8, a), __builtin_bit_cast(bf16x8, b), c, 0, 0, 0); }
__device__ __forceinline__ v2u tr_read(lptr p) { s16x4 r = __builtin_amdgcn_ds_read_tr16_b64_v4i16((LAS s16x4*)p); return __builtin_bit_cast(v2u, r); }
__device__ __forceinline__ v4u tr_frag(lptr p0, lptr p1) { v2u a = tr_read(p0), b = tr_read(p1); return v4u{a.x, a.y, b.x, b.y}; }
__device__ __forceinline__ v4u lds128(lptr p) { return *(LAS v4u*)p; }
__device__ __forceinline__ void bar() { asm volatile("s_waitcnt lgkmcnt(0)\n\ts_barrier" ::: "memory"); }
__device__ __forceinline__ void glds16(const void* gsrc, unsigned lds_dst) {
  unsigned keep;
  lds_dst = __builtin_amdgcn_readfirstlane(lds_dst);
  asm volatile("s_mov_b32 %0, m0\n\ts_mov_b32 m0, %2\n\ts_nop 0\n\tglobal_load_lds_dwordx4 %1, off nt\n\ts_mov_b32 m0, %0" : "=&s"(keep) : "v"(gsrc), "s"(lds_dst) : "memory");
}
__device__ __forceinline__ unsigned nosent(float f) { return min(__float_as_uint(f), SENT - 1u); }
__device__ __forceinline__ unsigned nosentu(unsigned u) { return min(u, SENT - 1u); }
__device__ __forceinline__ unsigned umax4(v4u a) { return max(max(a.x, a.y), max(a.z, a.w)); }
__device__ __forceinline__ bool all_lanes(bool ok) { return __builtin_amdgcn_ballot_w64(!ok) == 0ull; }
__device__ __forceinline__ float sel4(f32x4 d, int g) { const bool o = (g & 1) != 0; const float a = o ? d[1] : d[0], b = o ? d[3] : d[2]; return (g & 2) ? b : a; }
__device__ __forceinline__ bool isnan_(float x) { return x != x; }
#define PIN(x) asm volatile("" : "+v"(x))
__device__ __forceinline__ float fast_rcp(float x) { return __builtin_amdgcn_rcpf(x); }
#define RSRC(p, n) __builtin_amdgcn_make_buffer_rsrc((void*)(p), 0, (int)(n), 0x00020000)
#define LD_SC1(off) __builtin_amdgcn_raw_buffer_load_b128(rws, (off), 0, AUX_SC1)
#define ST_SC1(v, off) __builtin_amdgcn_raw_buffer_store_b128((v), rws, (off), 0, AUX_SC1)
#define FENCE() __builtin_amdgcn_sched_barrier(0)
#define LD8_SC1(off) __builtin_amdgcn_raw_buffer_load_b64(rws, (off), 0, AUX_SC1)
#define GATHER8(N, V, OFF) do { \
    unsigned pend_ = 0; \
    _Pragma("unroll") for (int i = 0; i < (N); ++i) V[i] = LD8_SC1(OFF); \
    _Pragma("unroll") for (int i = 0; i < (N); ++i) pend_ |= all_lanes(max(V[i].x, V[i].y) != SENT) ? 0u : 1u << i; \
    for (unsigned spin_ = 0; pend_ != 0u && spin_ < SPIN_MAX; ++spin_) { \
      asm volatile("" ::: "memory"); \
      _Pragma("unroll") for (int i = 0; i < (N); ++i) if (pend_ & (1u << i)) V[i] = LD8_SC1(OFF); \
      _Pragma("unroll") for (int i = 0; i < (N); ++i) if (pend_ & (1u << i)) { if (all_lanes(max(V[i].x, V[i].y) != SENT)) pend_ &= ~(1u << i); } \
    } } while (0)
#define GATHER(N, V, OFF) do { \
    unsigned pend_ = 0; \
    _Pragma("unroll") for (int i = 0; i < (N); ++i) V[i] = LD_SC1(OFF); \
    _Pragma("unroll") for (int i = 0; i < (N); ++i) pend_ |= all_lanes(umax4(V[i]) != SENT) ? 0u : 1u << i; \
    for (unsigned spin_ = 0; pend_ != 0u && spin_ < SPIN_MAX; ++spin_) { \
      asm volatile("" ::: "memory"); \
      _Pragma("unroll") for (int i = 0; i < (N); ++i) if (pend_ & (1u << i)) V[i] = LD_SC1(OFF); \
      _Pragma("unroll") for (int i = 0; i < (N); ++i) if (pend_ & (1u << i)) { if (all_lanes(umax4(V[i]) != SENT)) pend_ &= ~(1u << i); } \
    } } while (0)
#define STAMP(i) do { } while (0)
#define DUMP() do { } while (0)

__device__ __forceinline__ int img_chunk(int lane, int row) { return ((((lane & 7) >> 1) ^ ((row >> 1) & 3)) << 1) | (lane & 1); }
__device__ __forceinline__ int frag_base(int lane, int ct) { const int g = lane >> 4, q = (lane >> 2) & 3; return (4 * g + q) * 128 + ((ct ^ ((2 * g + (q >> 1)) & 3)) * 32) + (lane & 3) * 8; }
__device__ __forceinline__ v4u w_frag(lptr base, int s) { return tr_frag(base + s * 4096, base + s * 4096 + 2048); }
__device__ __forceinline__ int act_off(int k) { return (k >> 5) * 64 + ((k >> 2) & 3) * 16 + ((k >> 4) & 1) * 8; }
__device__ __forceinline__ void ln_consts(float sum, float sumsq, float& mean_r, float& rden) {
  const float mean = sum * (1.f / 512.f);
  const float var = fmaxf(sumsq * (1.f / 512.f) - mean * mean, 0.f);
  mean_r = rb(mean);
  rden = fast_rcp(rb(__builtin_amdgcn_sqrtf(rb(rb(var) + 1e-5f))));
}
__device__ __forceinline__ float ln_mod(float x, float mean_r, float rden, float c1, float ss) {
  return rb(rb(rb(rb(x - mean_r) * rden) * c1) + ss);
}

__global__ void __launch_bounds__(NTHR, 2) adaln_block_kernel(Params p) {
  __shared__ __attribute__((aligned(16))) unsigned char smem[L_BYTES + (STAMPS ? 1024 : 0)];
  lptr L = (lptr)smem;
  const unsigned lds0 = (unsigned)(uintptr_t)L;
  const int tid = threadIdx.x, lane = tid & 63, w = __builtin_amdgcn_readfirstlane(tid >> 6);
  const int cu = blockIdx.x, X = cu & 7, J = cu >> 3;
  const int g = lane >> 4, n = lane & 15;
  const auto rws = RSRC(p.ws, WS_END);
  const auto rk = RSRC(p.k, KV_BYTES);
  const auto rv = RSRC(p.v, KV_BYTES);
  const bool roleM = J < J_H, roleH = J >= J_H, roleGR = J >= J_H && J < J_GR_END, roleB = J >= J_B && J < J_C, roleC = J >= J_C && J < J_C_END;
  const int kq = J & 3;
  STAMP(0);
  const int mg = J < J_G ? 2 * X + (J >> 2) : 16 + 4 * X + ((J - J_G) >> 2);
  {
    const int r0 = 16 * w + (lane >> 3), ch = img_chunk(lane, r0);
    if (roleM) {
      const GAS unsigned char* s = (const GAS unsigned char*)p.wm + ((size_t)(128 * kq + r0) * 3072 + 64 * mg + 8 * ch) * 2;
      glds16((const void*)s, lds0 + L_WM + w * 2048);
      glds16((const void*)(s + 8 * 3072 * 2), lds0 + L_WM + w * 2048 + 1024);
    }
    const GAS unsigned char* s = (const GAS unsigned char*)p.wq + ((size_t)(128 * kq + r0) * 4096 + 64 * (8 * X + (J >> 2)) + 8 * ch) * 2;
    glds16((const void*)s, lds0 + L_WQ + w * 2048);
    glds16((const void*)(s + 8 * 4096 * 2), lds0 + L_WQ + w * 2048 + 1024);
  }
  const unsigned long long ep = __hip_atomic_load((GAS unsigned long long*)p.ws, __ATOMIC_RELAXED, __HIP_MEMORY_SCOPE_AGENT);
  v2u cv = {0u, 0u};
  if (roleM && tid < 128) cv = *(const GAS v2u*)((const GAS unsigned char*)p.c + ((tid >> 5) * 512 + 128 * kq + 4 * (tid & 31)) * 2);
  asm volatile("s_barrier" ::: "memory");
  if (w == 1) {
    const v4u sent = {SENT, SENT, SENT, SENT};
    ST_SC1(sent, WS_QP + ((X * 4 + g) * 4 + kq) * 2048 + (J >> 2) * 256 + n * 16);
    ST_SC1(sent, WS_PART + cu * 1024 + lane * 16);
    if (lane < 2) ST_SC1(sent, WS_STAT + cu * 32 + lane * 16);
    if (roleB) {
#pragma unroll
      for (int e = 0; e < 4; ++e) ST_SC1(sent, WS_ATT + (2 * e + (lane >> 5)) * 32768 + ((X * 4 + ((lane >> 3) & 3)) * 512 + 64 * (J - J_B)) * 2 + (lane & 7) * 16);
    }
    if (roleGR && n < 8) ST_SC1(sent, WS_GATE + g * 4096 + (4 * X + (J - J_H)) * 128 + n * 16);
    if (roleC) {
#pragma unroll
      for (int e = 0; e < 8; ++e) ST_SC1(sent, WS_DOWN + (7 * X + (J - J_C)) * 8192 + e * 1024 + lane * 16);
    }
  }
  FENCE();
  const bool roleT = roleB || roleC;
  const unsigned kvbase = (((unsigned)(J >> 3) * 8u + (unsigned)X) * 8192u + (unsigned)(J & 7) * 1024u) * 256u + (unsigned)w * 4096u;
  const unsigned koff = (4 * (lane & 3) + ((lane >> 2) & 1)) * 256 + (lane >> 3) * 16;
  v4u kf[8][4];
#define K_ISSUE(t) do { unsigned ko_ = koff; asm volatile("" : "+v"(ko_)); _Pragma("unroll") for (int q_ = 0; q_ < 4; ++q_) kf[t][q_] = __builtin_amdgcn_raw_buffer_load_b128(rk, ko_ + (q_ >> 1) * 512 + (q_ & 1) * 128, kvbase + (t) * 32768, AUX_NT); FENCE(); } while (0)
  K_ISSUE(0);
  if (FIRST > 1) K_ISSUE(1);
  if (tid == 0) { *(LAS unsigned*)(L + M_FLAG) = 0u; *(LAS unsigned*)(L + M_CLR) = 0u; *(LAS unsigned*)(L + M_CNT) = 0u; *(LAS unsigned*)(L + M_CN2) = 0u; }
  const unsigned long long E = ep + 1ull;
  const unsigned ta = __builtin_amdgcn_readfirstlane((unsigned)E), tb2 = __builtin_amdgcn_readfirstlane(((unsigned)E * 0x9E3779B1u) ^ (unsigned)(E >> 32));
  if (roleM && tid < 128) {
    float cs[4] = {bf_lo(cv.x), bf_hi(cv.x), bf_lo(cv.y), bf_hi(cv.y)};
#pragma unroll
    for (int e = 0; e < 4; ++e) cs[e] = cs[e] / (1.f + expf(-cs[e]));
    *(LAS v2u*)(L + L_ACT + (tid >> 5) * RS + act_off(4 * (tid & 31))) = v2u{pk2(cs[0], cs[1]), pk2(cs[2], cs[3])};
  }
  STAMP(17);
  asm volatile("s_waitcnt vmcnt(%0)" ::"i"(4 * FIRST) : "memory");
  STAMP(18);
  if (w == 1) {
    if (lane == 0) ST_SC1((v4u{0u, ta, 0u, tb2}), WS_READY + cu * 16);
  }
  bar();
  if (w == 1 && lane == 0) *(volatile LAS unsigned*)(L + M_CLR) = 1u;
  STAMP(1);
  if (roleM && w < 4) {
    lptr ap = L + L_ACT + (lane & 3) * RS + g * 16;
    lptr wp = L + L_WM + frag_base(lane, w);
    f32x4 a0 = mfma(lds128(ap), w_frag(wp, 0), f32x4{0.f, 0.f, 0.f, 0.f}), a1 = mfma(lds128(ap + 64), w_frag(wp, 1), f32x4{0.f, 0.f, 0.f, 0.f});
    a0 = mfma(lds128(ap + 128), w_frag(wp, 2), a0); a1 = mfma(lds128(ap + 192), w_frag(wp, 3), a1);
    const float v = sel4(a0 + a1, g);
    const float vn = dpp<XOR1>(v);
    if ((n & 1) == 0) ST_SC1((v4u{__float_as_uint(v), ta, __float_as_uint(vn), tb2}), WS_MP + ((mg * 4 + kq) * 4 + g) * 512 + (8 * w + (n >> 1)) * 16);
    FENCE();
  }
  if (roleH && w == 0) {
    __builtin_amdgcn_s_setprio(2);
    const int eta = 8 * X + (J - J_H), b = (lane >> 2) & 3, pr = lane & 3;
    v4u xq[4];
#pragma unroll
    for (int i = 0; i < 4; ++i) xq[i] = *(const GAS v4u*)((const GAS unsigned char*)p.x + (g * 512 + 32 * n + 8 * i) * 2);
    const unsigned xp = *(const GAS unsigned*)((const GAS unsigned char*)p.x + (b * 512 + 8 * eta + 2 * pr) * 2);
    float s1 = 0.f, s2 = 0.f;
#pragma unroll
    for (int i = 0; i < 4; ++i)
#pragma unroll
      for (int e = 0; e < 4; ++e) { const float a = bf_lo(xq[i][e]), c = bf_hi(xq[i][e]); s1 += a + c; s2 += a * a + c * c; }
    s1 = row_sum(s1); s2 = row_sum(s2);
    float mean_r, rden;
    ln_consts(s1, s2, mean_r, rden);
    mean_r = __builtin_bit_cast(float, __builtin_amdgcn_ds_bpermute(64 * b, __builtin_bit_cast(int, mean_r)));
    rden = __builtin_bit_cast(float, __builtin_amdgcn_ds_bpermute(64 * b, __builtin_bit_cast(int, rden)));
    v4u ms, mc;
    for (unsigned spin = 0; spin < SPIN_MAX; ++spin) {
      ms = LD_SC1(WS_MP + (((eta >> 3) * 4 + g) * 4 + b) * 512 + ((eta & 7) * 4 + pr) * 16);
      mc = LD_SC1(WS_MP + (((8 + (eta >> 3)) * 4 + g) * 4 + b) * 512 + ((eta & 7) * 4 + pr) * 16);
      if (all_lanes((ms.y == ta) & (ms.w == tb2) & (mc.y == ta) & (mc.w == tb2))) break;
      asm volatile("" ::: "memory");
    }
    const float sa0 = rb(x32_sum(x16_sum(__uint_as_float(ms.x)))), sa1 = rb(x32_sum(x16_sum(__uint_as_float(ms.z))));
    const float ca0 = rb(x32_sum(x16_sum(__uint_as_float(mc.x)))), ca1 = rb(x32_sum(x16_sum(__uint_as_float(mc.z))));
    const unsigned hp = pk2(ln_mod(bf_lo(xp), mean_r, rden, rb(1.f + ca0), sa0), ln_mod(bf_hi(xp), mean_r, rden, rb(1.f + ca1), sa1));
    const unsigned hn = dppu<XOR1>(hp);
    if (lane < 16 && (pr & 1) == 0) ST_SC1((v4u{hp, ta, hn, tb2}), WS_H + eta * 128 + (b * 2 + (pr >> 1)) * 16);
    __builtin_amdgcn_s_setprio(0);
    FENCE();
  }
  STAMP(2);
#define PACE_WAITN(x) do { if (w != 0 && PACE >= 0) asm volatile("s_waitcnt vmcnt(%0)" ::"i"(PACE < 0 ? 0 : PACE + (x)) : "memory"); } while (0)
#define PACE_A() do { if (w != 0) asm volatile("s_waitcnt vmcnt(%0)" ::"i"(PA) : "memory"); } while (0)
#define PACE_B() do { if (w != 0) asm volatile("s_waitcnt vmcnt(%0)" ::"i"(PB) : "memory"); } while (0)
#define PACE_WAIT2() PACE_WAITN(4)
#define PACE_WAIT() PACE_WAITN(0)
  if (FIRST < 2) K_ISSUE(1);

  PACE_A(); K_ISSUE(2); PACE_A(); K_ISSUE(3);
  if (w == 0) {
    __builtin_amdgcn_s_setprio(2);
    v4u bq[4][4];
#pragma unroll
    for (int ct = 0; ct < 4; ++ct) { lptr wp = L + L_WQ + frag_base(lane, ct);
#pragma unroll
      for (int s = 0; s < 4; ++s) bq[ct][s] = w_frag(wp, s); }
    v4u hv[2];
    for (unsigned spin = 0; spin < SPIN_MAX; ++spin) {
      bool ok = true;
#pragma unroll
      for (int i = 0; i < 2; ++i) { hv[i] = LD_SC1(WS_H + (128 * kq + 64 * i + lane) * 16); ok &= (hv[i].y == ta) & (hv[i].w == tb2); }
#pragma unroll
      for (int i = 0; i < 4; ++i) { const v4u rd = LD_SC1(WS_READY + (64 * i + lane) * 16); ok &= (rd.y == ta) & (rd.w == tb2); }
      if (all_lanes(ok)) break;
      asm volatile("" ::: "memory");
    }
    STAMP(3);
#pragma unroll
    for (int i = 0; i < 2; ++i) { const int gm = 64 * i + lane; *(LAS v2u*)(L + L_ACTH + ((gm >> 1) & 3) * RS + act_off(8 * (gm >> 3) + 4 * (gm & 1))) = v2u{hv[i].x, hv[i].z}; }
    asm volatile("s_waitcnt lgkmcnt(0)" ::: "memory");
    lptr ap = L + L_ACTH + (lane & 3) * RS + g * 16;
    const v4u a0 = lds128(ap), a1 = lds128(ap + 64), a2 = lds128(ap + 128), a3 = lds128(ap + 192);
    float qv[4];
#pragma unroll
    for (int ct = 0; ct < 4; ++ct) {
      const f32x4 z = {0.f, 0.f, 0.f, 0.f};
      qv[ct] = sel4(mfma(a1, bq[ct][1], mfma(a0, bq[ct][0], z)) + mfma(a3, bq[ct][3], mfma(a2, bq[ct][2], z)), g);
    }
    while (*(volatile LAS unsigned*)(L + M_CLR) == 0u) __builtin_amdgcn_s_sleep(1);
    ST_SC1((v4u{nosent(qv[0]), nosent(qv[1]), nosent(qv[2]), nosent(qv[3])}), WS_QP + ((X * 4 + g) * 4 + kq) * 2048 + (J >> 2) * 256 + n * 16);
    if (cu == 0 && lane == 0) __hip_atomic_store((GAS unsigned long long*)p.ws, E, __ATOMIC_RELAXED, __HIP_MEMORY_SCOPE_AGENT);
    __builtin_amdgcn_s_setprio(0);
    FENCE();
    STAMP(4);
  }
  PACE_B(); K_ISSUE(4); PACE_B(); K_ISSUE(5);
  PACE_WAIT(); K_ISSUE(6); PACE_WAIT(); K_ISSUE(7);
  if (w == 0) {
    __builtin_amdgcn_s_setprio(2);
    v4u qp[8];
    for (unsigned spin = 0; spin < SPIN_MAX; ++spin) {
      unsigned m = 0;
#pragma unroll
      for (int i = 0; i < 8; ++i) { qp[i] = LD_SC1(WS_QP + ((X * 4 + (J >> 3)) * 4 + (i >> 1)) * 2048 + ((i & 1) * 64 + lane) * 16); m = max(m, umax4(qp[i])); }
      if (all_lanes(m != SENT)) break;
      asm volatile("" ::: "memory");
    }
#pragma unroll
    for (int hf = 0; hf < 2; ++hf)
#pragma unroll
      for (int ct = 0; ct < 4; ++ct) {
        const float s = (__uint_as_float(qp[hf][ct]) + __uint_as_float(qp[2 + hf][ct])) + (__uint_as_float(qp[4 + hf][ct]) + __uint_as_float(qp[6 + hf][ct]));
        *(LAS unsigned short*)(L + L_Q + (64 * (4 * hf + g) + 16 * ct + n) * 2) = (unsigned short)(pk2(s, s) & 0xffffu);
      }
    asm volatile("s_waitcnt lgkmcnt(0)" ::: "memory");
    STAMP(5);
    if (roleGR) {
      const int gg = 4 * X + (J - J_H);
      v4u gv[8];
      for (unsigned spin = 0; spin < SPIN_MAX; ++spin) {
        bool ok = true;
#pragma unroll
        for (int i = 0; i < 8; ++i) { gv[i] = LD_SC1(WS_MP + (((16 + gg) * 4 + (i >> 1)) * 4 + g) * 512 + ((i & 1) * 16 + n) * 16); ok &= (gv[i].y == ta) & (gv[i].w == tb2); }
        if (all_lanes(ok)) break;
        asm volatile("" ::: "memory");
      }
#pragma unroll
      for (int hf = 0; hf < 2; ++hf) {
        const float s0 = (__uint_as_float(gv[hf].x) + __uint_as_float(gv[2 + hf].x)) + (__uint_as_float(gv[4 + hf].x) + __uint_as_float(gv[6 + hf].x));
        const float s1 = (__uint_as_float(gv[hf].z) + __uint_as_float(gv[2 + hf].z)) + (__uint_as_float(gv[4 + hf].z) + __uint_as_float(gv[6 + hf].z));
        __builtin_amdgcn_raw_buffer_store_b32(nosentu(pk2(s0, s1)), rws, WS_GATE + g * 4096 + gg * 128 + (16 * hf + n) * 4, 0, AUX_SC1);
      }
    }
    __builtin_amdgcn_s_setprio(0);
    FENCE();
  }
  v4u vf[4][8];
#define V_ISSUE(u) do { unsigned vo_ = lane * 16; asm volatile("" : "+v"(vo_)); _Pragma("unroll") for (int j_ = 0; j_ < 8; ++j_) vf[u][j_] = __builtin_amdgcn_raw_buffer_load_b128(rv, vo_ + (j_ & 3) * 1024, kvbase + (2 * (u) + (j_ >> 2)) * 32768, AUX_NT); FENCE(); } while (0)
  PACE_WAIT(); V_ISSUE(0);
  PACE_WAIT2(); V_ISSUE(1);
  STAMP(6);
  bar();
  STAMP(7);
  const v4u qf0 = lds128(L + L_Q + (n & 3) * 256 + g * 32 + ((n >> 2) & 1) * 16), qf1 = lds128(L + L_Q + (n & 3) * 256 + 128 + g * 32 + ((n >> 2) & 1) * 16);
  float S[8][4];
#define QK(t) do { const f32x4 z_ = {0.f, 0.f, 0.f, 0.f}; \
    const f32x4 d0_ = mfma(kf[t][1], qf1, mfma(kf[t][0], qf0, z_)), d1_ = mfma(kf[t][3], qf1, mfma(kf[t][2], qf0, z_)); \
    _Pragma("unroll") for (int r_ = 0; r_ < 4; ++r_) { \
      auto s_ = __builtin_amdgcn_permlane32_swap(__float_as_uint(d0_[r_]), __float_as_uint(d1_[r_]), false, false); \
      S[t][r_] = __uint_as_float(s_[0]) + dpp<ROR4>(__uint_as_float(s_[1])); } } while (0)
  QK(0); QK(1); FENCE(); V_ISSUE(2);
  QK(2); QK(3); FENCE(); V_ISSUE(3);
  if (W_GATE && lane == 0) (void)__hip_atomic_fetch_add((LAS unsigned*)(L + M_CN2), 1u, __ATOMIC_RELAXED, __HIP_MEMORY_SCOPE_WORKGROUP);
  QK(4); QK(5); QK(6); QK(7); FENCE();
  STAMP(8);
  unsigned S2[8][2];
  float mx = -__builtin_inff();
#pragma unroll
  for (int t = 0; t < 8; ++t)
#pragma unroll
    for (int r = 0; r < 2; ++r) {
      const unsigned a = pk2(S[t][2 * r], S[t][2 * r + 1]);
      S2[t][r] = pk2(bf_lo(a) * 0.08838834764831845f, bf_hi(a) * 0.08838834764831845f);
      mx = fmaxf(mx, fmaxf(bf_lo(S2[t][r]), bf_hi(S2[t][r])));
    }
  FENCE();
  FENCE();
  mx = x32_max(x16_max(mx));
  float ls = 0.f;
  v4u pf[4];
#pragma unroll
  for (int u = 0; u < 4; ++u) {
    float e[8];
#pragma unroll
    for (int j = 0; j < 8; ++j) { const unsigned sp = S2[2 * u + (j >> 2)][(j & 3) >> 1]; e[j] = __builtin_amdgcn_exp2f((((j & 1) ? bf_hi(sp) : bf_lo(sp)) - mx) * LOG2E); ls += e[j]; }
    pf[u] = v4u{pk2(e[0], e[1]), pk2(e[2], e[3]), pk2(e[4], e[5]), pk2(e[6], e[7])};
  }
  ls = x32_sum(x16_sum(ls));
  if (lane < 4) { *(LAS float*)(L + M_MX + lane * 32 + w * 4) = mx; *(LAS float*)(L + M_LS + lane * 32 + w * 4) = ls; }
  {
    asm volatile("s_waitcnt lgkmcnt(0)" ::: "memory");
    unsigned old = 0;
    if (lane == 0) old = __hip_atomic_fetch_add((LAS unsigned*)(L + M_CNT), 1u, __ATOMIC_RELAXED, __HIP_MEMORY_SCOPE_WORKGROUP);
    if (__builtin_amdgcn_readfirstlane(old) == 7u) {
      const float mw = *(volatile LAS float*)(L + M_MX + ((lane >> 3) & 3) * 32 + (lane & 7) * 4), lw = *(volatile LAS float*)(L + M_LS + ((lane >> 3) & 3) * 32 + (lane & 7) * 4);
      float M = fmaxf(mw, dpp<XOR1>(mw)); M = fmaxf(M, dpp<XOR2>(M)); M = fmaxf(M, dpp<HMIR>(M));
      const float f = __builtin_amdgcn_exp2f((mw - M) * LOG2E);
      float Lc = f * lw; Lc += dpp<XOR1>(Lc); Lc += dpp<XOR2>(Lc); Lc += dpp<HMIR>(Lc);
      if (lane < 32) *(LAS float*)(L + M_FW + lane * 4) = f;
      if (lane < 32 && (lane & 7) == 0) __builtin_amdgcn_raw_buffer_store_b64((v2u{nosent(M), nosent(Lc)}), rws, WS_STAT + cu * 32 + (lane >> 3) * 8, 0, AUX_SC1);
    }
  }
  FENCE();
  STAMP(9);
  v4u wr[8];
#define W_LOADS() do { \
     \
    const int pc_ = 7 * X + (J - J_C); \
    const auto rwa = RSRC(roleB ? p.wo : p.wg, roleB ? 4096 * 512 * 2 : roleC ? 512 * 1792 * 2 : 0); \
    const auto rwb = RSRC(roleB ? p.wo : p.wu, roleB ? 4096 * 512 * 2 : roleC ? 512 * 1792 * 2 : 0); \
    const int rB = 64 * w + (lane >> 3); \
    const int rS = 128 * (w >> 1) + 32 * ((lane >> 5) & 1) + 8 * ((lane >> 3) & 3) + 4 * (w & 1); \
    const unsigned vo = roleB ? ((512 * X + rS) * 512 + 64 * (J - J_B) + 8 * img_chunk(lane, rB)) * 2 : ((64 * w + (lane >> 2)) * 1792 + 32 * pc_ + 8 * (lane & 3)) * 2; \
_Pragma("unroll") \
    for (int e = 0; e < 4; ++e) wr[e] = __builtin_amdgcn_raw_buffer_load_b128(rwa, vo, roleB ? (64 * (e & 1) + (e >> 1)) * 1024 : e * 16 * 1792 * 2, AUX_NT); \
_Pragma("unroll") \
    for (int e = 0; e < 4; ++e) wr[4 + e] = __builtin_amdgcn_raw_buffer_load_b128(rwb, vo, roleB ? (64 * (e & 1) + 2 + (e >> 1)) * 1024 : e * 16 * 1792 * 2, AUX_NT); \
  } while (0)
  f32x4 oa[8];
#pragma unroll
  for (int d = 0; d < 8; ++d) oa[d] = f32x4{0.f, 0.f, 0.f, 0.f};
#define PV(u) do { _Pragma("unroll") for (int d = 0; d < 8; ++d) { \
      const unsigned sel = (d & 1) ? 0x07060302u : 0x05040100u; v4u a; \
      _Pragma("unroll") for (int i = 0; i < 4; ++i) a[i] = __builtin_amdgcn_perm(vf[u][2 * i + 1][d >> 1], vf[u][2 * i][d >> 1], sel); \
      oa[d] = mfma(a, pf[u], oa[d]); } FENCE(); } while (0)
  PV(0); PV(1);
  if (W_AT == 1) W_LOADS();
  PV(2);
  if (W_AT == 2) W_LOADS();
  PV(3);
  if (W_AT == 3) {
    if (W_GATE && roleT) { for (unsigned spin = 0; spin < (SPIN_MAX << 4); ++spin) { if (*(volatile LAS unsigned*)(L + M_CN2) >= 8u) break; __builtin_amdgcn_s_sleep(1); } }
    W_LOADS(); FENCE();
  }
  STAMP(10);
  int tid2 = threadIdx.x; asm volatile("" : "+v"(tid2));
#define tid tid2
  const int lane2 = tid2 & 63;
#define lane lane2
  const int g2 = lane2 >> 4, n2 = lane2 & 15;
#define g g2
#define n n2
  const int tb = tid2 >> 7, tk = 4 * (tid2 & 127);
  if (n < 4) {
#pragma unroll
    for (int d = 0; d < 8; ++d) *(LAS f32x4*)(L + L_MRG + w * MRG_W + n * MRG_H + d * 64 + g * 16) = oa[d];
  }
  bar();
  {
    const f32x4 f0 = *(LAS f32x4*)(L + M_FW + tb * 32), f1 = *(LAS f32x4*)(L + M_FW + tb * 32 + 16);
    float o = 0.f;
#pragma unroll
    for (int ww = 0; ww < 8; ++ww) o += (ww < 4 ? f0[ww & 3] : f1[ww & 3]) * *(LAS float*)(L + L_MRG + ww * MRG_W + tb * MRG_H + (tid & 127) * 4);
    const float on = dpp<XOR1>(o);
    if ((tid & 1) == 0) __builtin_amdgcn_raw_buffer_store_b32(nosentu(pk2(o, on)), rws, WS_PART + cu * 1024 + tid * 2, 0, AUX_SC1);
  }
  STAMP(11);
  if (!(roleB || roleC)) { STAMP(16); DUMP(); return; }
  v4u wdr[4], gt[2];
  v2u xr;
  if (roleC) {
#pragma unroll
    for (int e = 0; e < 2; ++e) gt[e] = LD_SC1(WS_GATE + ((2 * w + e) * 64 + lane) * 16);
    xr = *(const GAS v2u*)((const GAS unsigned char*)p.x + tid * 8);
    const auto rd = RSRC(p.wd, 1792 * 512 * 2);
    const unsigned o = ((32 * (7 * X + (J - J_C)) + (lane >> 3)) * 512 + 64 * w + 8 * img_chunk(lane, lane >> 3)) * 2;
#pragma unroll
    for (int e = 0; e < 4; ++e) wdr[e] = __builtin_amdgcn_raw_buffer_load_b128(rd, o, e * 8 * 512 * 2, AUX_NT);
  }
  if (roleB) {
#pragma unroll
    for (int e = 0; e < 8; ++e) *(LAS v4u*)(L + L_W + w * 8192 + e * 1024 + lane * 16) = wr[e];
  } else {
    const int rw = lane >> 2, x = (rw >> 1) & 3, c = lane & 3;
    lptr pg = L + L_W + w * 8192 + rw * 128 + ((((c >> 1) ^ x) << 1) | (c & 1)) * 16, pu = L + L_W + w * 8192 + rw * 128 + ((((2 + (c >> 1)) ^ x) << 1) | (c & 1)) * 16;
#pragma unroll
    for (int e = 0; e < 4; ++e) { *(LAS v4u*)(pg + e * 2048) = wr[e]; *(LAS v4u*)(pu + e * 2048) = wr[4 + e]; }
  }
  if (roleB) {
    const int bb = w >> 1, hf = lane >> 5;
    v2u st[2];
    for (unsigned spin = 0; spin < SPIN_MAX; ++spin) {
#pragma unroll
      for (int hh = 0; hh < 2; ++hh) st[hh] = __builtin_amdgcn_raw_buffer_load_b64(rws, WS_STAT + ((8 * bb + 2 * (lane & 3) + hf) * 8 + X) * 32 + (2 * (w & 1) + hh) * 8, 0, AUX_SC1);
      if (all_lanes(max(max(st[0].x, st[0].y), max(st[1].x, st[1].y)) != SENT)) break;
      asm volatile("" ::: "memory");
    }
    float wt[2][4];
#pragma unroll
    for (int hh = 0; hh < 2; ++hh) {
      const float ms = __uint_as_float(st[hh].x);
      float M = fmaxf(ms, dpp<XOR1>(ms)); M = fmaxf(M, dpp<XOR2>(M)); M = x32_max(M);
      const float f = __builtin_amdgcn_exp2f((ms - M) * LOG2E);
      float Ls = f * __uint_as_float(st[hh].y); Ls += dpp<XOR1>(Ls); Ls += dpp<XOR2>(Ls); Ls = x32_sum(Ls);
      const float x = f * fast_rcp(Ls);
      wt[hh][0] = dpp<0x00>(x); wt[hh][1] = dpp<0x55>(x); wt[hh][2] = dpp<0xAA>(x); wt[hh][3] = dpp<0xFF>(x);
    }
    asm volatile("s_waitcnt lgkmcnt(0)" ::: "memory");
    v4u bo[2][4];
#pragma unroll
    for (int ct = 0; ct < 4; ++ct) { lptr wp = L + L_W + w * 8192 + frag_base(lane, ct); bo[0][ct] = w_frag(wp, 0); bo[1][ct] = w_frag(wp, 1); }
    int o_act = L_ACT + bb * RS + act_off(256 * (w & 1) + 4 * (lane & 31));
    int o_a = L_ACT + (lane & 3) * RS + g * 16 + (2 * w) * 64;
    int o_red = L_RED + w * 1024 + g * 256 + n * 4, o_rd = L_RED + lane * 16;
    int o_src = WS_PART + ((8 * bb + hf) * 8 + X) * 1024 + (w & 1) * 512 + (lane & 31) * 8;
    int o_dst = WS_ATT + w * 32768 + ((X * 4 + g) * 512 + 64 * (J - J_B) + 4 * n) * 2;
    PIN(o_act); PIN(o_a); PIN(o_red); PIN(o_rd); PIN(o_src); PIN(o_dst);
    STAMP(19);
    float o[2][4];
    {
      v2u po[8];
      GATHER8(8, po, o_src + (i & 3) * 2 * 8 * 1024 + (i >> 2) * 256);
#pragma unroll
      for (int hh = 0; hh < 2; ++hh) {
        o[hh][0] = x32_sum(wt[hh][0] * bf_lo(po[4 * hh].x) + wt[hh][1] * bf_lo(po[4 * hh + 1].x) + wt[hh][2] * bf_lo(po[4 * hh + 2].x) + wt[hh][3] * bf_lo(po[4 * hh + 3].x));
        o[hh][1] = x32_sum(wt[hh][0] * bf_hi(po[4 * hh].x) + wt[hh][1] * bf_hi(po[4 * hh + 1].x) + wt[hh][2] * bf_hi(po[4 * hh + 2].x) + wt[hh][3] * bf_hi(po[4 * hh + 3].x));
        o[hh][2] = x32_sum(wt[hh][0] * bf_lo(po[4 * hh].y) + wt[hh][1] * bf_lo(po[4 * hh + 1].y) + wt[hh][2] * bf_lo(po[4 * hh + 2].y) + wt[hh][3] * bf_lo(po[4 * hh + 3].y));
        o[hh][3] = x32_sum(wt[hh][0] * bf_hi(po[4 * hh].y) + wt[hh][1] * bf_hi(po[4 * hh + 1].y) + wt[hh][2] * bf_hi(po[4 * hh + 2].y) + wt[hh][3] * bf_hi(po[4 * hh + 3].y));
      }
    }
    STAMP(12);
    if (lane < 32) {
      *(LAS v2u*)(L + o_act) = v2u{pk2(o[0][0], o[0][1]), pk2(o[0][2], o[0][3])};
      *(LAS v2u*)(L + o_act + 256) = v2u{pk2(o[1][0], o[1][1]), pk2(o[1][2], o[1][3])};
    }
    bar();
    {
      const v4u a0 = lds128(L + o_a), a1 = lds128(L + o_a + 64);
#pragma unroll
      for (int ct = 0; ct < 4; ++ct) {
        f32x4 d = mfma(a0, bo[0][ct], f32x4{0.f, 0.f, 0.f, 0.f});
        d = mfma(a1, bo[1][ct], d);
        *(LAS float*)(L + o_red + ct * 64) = sel4(d, g);
      }
    }
    bar();
    {
      f32x4 s0 = *(LAS f32x4*)(L + o_rd), s1 = *(LAS f32x4*)(L + o_rd + 1024);
#pragma unroll
      for (int ww = 2; ww < 8; ww += 2) { s0 += *(LAS f32x4*)(L + o_rd + ww * 1024); s1 += *(LAS f32x4*)(L + o_rd + ww * 1024 + 1024); }
      s0 += s1;
      __builtin_amdgcn_raw_buffer_store_b64((v2u{nosentu(pk2(s0.x, s0.y)), nosentu(pk2(s0.z, s0.w))}), rws, o_dst, 0, AUX_SC1);
    }
    STAMP(13); STAMP(16); DUMP();
    return;
  }
  const int pc = 7 * X + (J - J_C);
#pragma unroll
  for (int e = 0; e < 4; ++e) *(LAS v4u*)(L + L_WD + w * 4096 + e * 1024 + lane * 16) = wdr[e];
  if (!all_lanes(umax4(gt[0]) != SENT && umax4(gt[1]) != SENT)) {
    for (unsigned spin = 0; spin < SPIN_MAX; ++spin) {
      asm volatile("" ::: "memory");
#pragma unroll
      for (int e = 0; e < 2; ++e) gt[e] = LD_SC1(WS_GATE + ((2 * w + e) * 64 + lane) * 16);
      if (all_lanes(umax4(gt[0]) != SENT && umax4(gt[1]) != SENT)) break;
    }
  }
#pragma unroll
  for (int e = 0; e < 2; ++e) *(LAS v4u*)(L + L_GATE + ((2 * w + e) * 64 + lane) * 16) = gt[e];
  if (tid < 4) *(LAS v4u*)(L + M_ZERO + tid * 16) = v4u{0u, 0u, 0u, 0u};
  bar();
  {
    const v2u ga = *(LAS v2u*)(L + L_GATE + tb * 4096 + tk * 2), sb = *(LAS v2u*)(L + L_GATE + tb * 4096 + 1024 + tk * 2), cb = *(LAS v2u*)(L + L_GATE + tb * 4096 + 2048 + tk * 2);
    const v2u xw = xr;
    const float gav[4] = {bf_lo(ga.x), bf_hi(ga.x), bf_lo(ga.y), bf_hi(ga.y)}, sbv[4] = {bf_lo(sb.x), bf_hi(sb.x), bf_lo(sb.y), bf_hi(sb.y)},
                c1v[4] = {rb(1.f + bf_lo(cb.x)), rb(1.f + bf_hi(cb.x)), rb(1.f + bf_lo(cb.y)), rb(1.f + bf_hi(cb.y))}, xs[4] = {bf_lo(xw.x), bf_hi(xw.x), bf_lo(xw.y), bf_hi(xw.y)};
    v4u bg[2][4], bd[4];
#pragma unroll
    for (int ct = 0; ct < 4; ++ct) { lptr wp = L + L_W + w * 8192 + frag_base(lane, ct); bg[0][ct] = w_frag(wp, 0); bg[1][ct] = w_frag(wp, 1); }
    {
      const int q = (lane >> 2) & 3;
#pragma unroll
      for (int kp = 0; kp < 4; ++kp) {
        const int ra = 8 * kp + q, rb_ = ra + 4;
        bd[kp] = tr_frag(L + L_WD + w * 4096 + ra * 128 + ((g ^ ((ra >> 1) & 3)) * 32) + (lane & 3) * 8, L + L_WD + w * 4096 + rb_ * 128 + ((g ^ ((rb_ >> 1) & 3)) * 32) + (lane & 3) * 8);
      }
    }
    int o_src = WS_ATT + X * 32768 + (tb * 512 + tk) * 2;
    int o_x = L_X + tid * 8, o_ln = M_LN + tb * 16, o_act = L_ACT + tb * RS + act_off(tk), o_a = L_ACT + (lane & 3) * RS + g * 16 + (2 * w) * 64;
    int o_red = L_RED + w * 1024 + g * 256 + n * 4, o_rd = L_RED + (2 * w + (lane >> 5)) * 256 + (lane & 31) * 4;
    int o_hw = M_HID + (2 * w + (lane >> 5)) * 64 + (lane & 31) * 2, o_hr = (n >> 2) == g ? M_HID + (n & 3) * 64 : M_ZERO;
    int o_dst = WS_DOWN + pc * 8192 + tid * 16;
    PIN(o_src); PIN(o_x); PIN(o_ln); PIN(o_act); PIN(o_a); PIN(o_red); PIN(o_rd); PIN(o_hw); PIN(o_hr); PIN(o_dst);
    float gv[4] = {gav[0], gav[1], gav[2], gav[3]}, sv[4] = {sbv[0], sbv[1], sbv[2], sbv[3]}, cv1[4] = {c1v[0], c1v[1], c1v[2], c1v[3]}, xx[4] = {xs[0], xs[1], xs[2], xs[3]};
#pragma unroll
    for (int e = 0; e < 4; ++e) { PIN(gv[e]); PIN(sv[e]); PIN(cv1[e]); PIN(xx[e]); }
    STAMP(19);
    float at[4];
    {
      v2u av[8];
      GATHER8(8, av, o_src + i * 4 * 1024);
      at[0] = ((bf_lo(av[0].x) + bf_lo(av[1].x)) + (bf_lo(av[2].x) + bf_lo(av[3].x))) + ((bf_lo(av[4].x) + bf_lo(av[5].x)) + (bf_lo(av[6].x) + bf_lo(av[7].x)));
      at[1] = ((bf_hi(av[0].x) + bf_hi(av[1].x)) + (bf_hi(av[2].x) + bf_hi(av[3].x))) + ((bf_hi(av[4].x) + bf_hi(av[5].x)) + (bf_hi(av[6].x) + bf_hi(av[7].x)));
      at[2] = ((bf_lo(av[0].y) + bf_lo(av[1].y)) + (bf_lo(av[2].y) + bf_lo(av[3].y))) + ((bf_lo(av[4].y) + bf_lo(av[5].y)) + (bf_lo(av[6].y) + bf_lo(av[7].y)));
      at[3] = ((bf_hi(av[0].y) + bf_hi(av[1].y)) + (bf_hi(av[2].y) + bf_hi(av[3].y))) + ((bf_hi(av[4].y) + bf_hi(av[5].y)) + (bf_hi(av[6].y) + bf_hi(av[7].y)));
    }
    STAMP(14);
    unsigned xp[2];
    float x2[4];
#pragma unroll
    for (int e = 0; e < 4; e += 2) {
      const unsigned a = pk2(at[e], at[e + 1]);
      const unsigned t = pk2(gv[e] * bf_lo(a), gv[e + 1] * bf_hi(a));
      xp[e >> 1] = pk2(xx[e] + bf_lo(t), xx[e + 1] + bf_hi(t));
      x2[e] = bf_lo(xp[e >> 1]); x2[e + 1] = bf_hi(xp[e >> 1]);
    }
    float s1 = (x2[0] + x2[1]) + (x2[2] + x2[3]), s2 = (x2[0] * x2[0] + x2[1] * x2[1]) + (x2[2] * x2[2] + x2[3] * x2[3]);
    wave_sum2(s1, s2);
    if (lane == 0) *(LAS v2u*)(L + M_LN + w * 8) = v2u{__float_as_uint(s1), __float_as_uint(s2)};
    *(LAS v2u*)(L + o_x) = v2u{xp[0], xp[1]};
    bar();
    STAMP(20);
    {
      const f32x4 s = *(LAS f32x4*)(L + o_ln);
      float mean_r, rden;
      ln_consts(s.x + s.z, s.y + s.w, mean_r, rden);
      unsigned hp[2];
#pragma unroll
      for (int e = 0; e < 4; e += 2) {
        const unsigned a = pk2(x2[e] - mean_r, x2[e + 1] - mean_r);
        const unsigned t = pk2(bf_lo(a) * rden, bf_hi(a) * rden);
        const unsigned u = pk2(bf_lo(t) * cv1[e], bf_hi(t) * cv1[e + 1]);
        hp[e >> 1] = pk2(bf_lo(u) + sv[e], bf_hi(u) + sv[e + 1]);
      }
      *(LAS v2u*)(L + o_act) = v2u{hp[0], hp[1]};
    }
    bar();
    STAMP(21);
    {
      const v4u a0 = lds128(L + o_a), a1 = lds128(L + o_a + 64);
#pragma unroll
      for (int ct = 0; ct < 4; ++ct) {
        f32x4 d = mfma(a0, bg[0][ct], f32x4{0.f, 0.f, 0.f, 0.f});
        d = mfma(a1, bg[1][ct], d);
        *(LAS float*)(L + o_red + ct * 64) = sel4(d, g);
      }
    }
    bar();
    STAMP(22);
    if (w < 2) {
      float p0[8], p1[8];
#pragma unroll
      for (int ww = 0; ww < 8; ++ww) { p0[ww] = *(LAS float*)(L + o_rd + ww * 1024); p1[ww] = *(LAS float*)(L + o_rd + ww * 1024 + 128); }
      const unsigned gu = pk2(((p0[0] + p0[1]) + (p0[2] + p0[3])) + ((p0[4] + p0[5]) + (p0[6] + p0[7])), ((p1[0] + p1[1]) + (p1[2] + p1[3])) + ((p1[4] + p1[5]) + (p1[6] + p1[7])));
      const float sg = bf_lo(gu), up = bf_hi(gu);
      const float u = sg * (1.f + 0.044715f * sg * sg) * (-2.f * LOG2E * 0.7978845608028654f);
      const float ge = rb(sg * fast_rcp(1.f + __builtin_amdgcn_exp2f(u)));
      *(LAS unsigned short*)(L + o_hw) = (unsigned short)(pk2(ge * up, 0.f) & 0xffffu);
    }
    bar();
    STAMP(23);
    {
      const v4u h0 = lds128(L + o_hr), h1 = lds128(L + o_hr + 16), h2 = lds128(L + o_hr + 32), h3 = lds128(L + o_hr + 48);
      const f32x4 z = {0.f, 0.f, 0.f, 0.f};
      const f32x4 d = mfma(h1, bd[1], mfma(h0, bd[0], z)) + mfma(h3, bd[3], mfma(h2, bd[2], z));
      ST_SC1((v4u{nosent(d[0]), nosent(d[1]), nosent(d[2]), nosent(d[3])}), o_dst);
    }
  }
  STAMP(15);
  if (J < J_F_END) {
    const int f = 4 * X + (J - J_C);
    float gb = bf_lo(*(LAS unsigned short*)(L + L_GATE + g * 4096 + 3072 + (16 * f + n) * 2));
    float x2 = bf_lo(*(LAS unsigned short*)(L + L_X + (g * 512 + 16 * f + n) * 2));
    int o_src = WS_DOWN + (4 * w + g) * 8192 + (16 * f + n) * 16, o_wr = L_RED + w * 256 + lane * 16, o_rd = L_RED + n * 16 + g * 4, o_out = (g * 512 + 16 * f + n) * 2;
    PIN(gb); PIN(x2); PIN(o_src); PIN(o_wr); PIN(o_rd); PIN(o_out);
    if (w < 7) {
      f32x4 s;
      v4u dv[2];
      GATHER(2, dv, o_src + i * 28 * 8192);
#pragma unroll
      for (int b = 0; b < 4; ++b) s[b] = x32_sum(x16_sum(__uint_as_float(dv[0][b]) + __uint_as_float(dv[1][b])));
      if (lane < 16) *(LAS f32x4*)(L + o_wr) = s;
    }
    bar();
    if (w == 0) {
      float q7[7];
#pragma unroll
      for (int ww = 0; ww < 7; ++ww) q7[ww] = *(LAS float*)(L + o_rd + ww * 256);
      const float y = rb(((q7[0] + q7[1]) + (q7[2] + q7[3])) + ((q7[4] + q7[5]) + q7[6]));
      const unsigned ob = pk2(x2 + rb(gb * y), 0.f) & 0xffffu;
      const unsigned o2 = ob | (dppu<XOR1>(ob) << 16);
      if ((n & 1) == 0) *(GAS unsigned*)((GAS unsigned char*)p.out + o_out) = o2;
    }
  }
  STAMP(16); DUMP();
#undef tid
#undef lane
#undef g
#undef n
}
}

extern "C" void kernel_launch(void* const* d_in, const int* in_sizes, int n_in, void* d_out, int out_size, void* d_ws, size_t ws_size, hipStream_t stream) {
  (void)in_sizes; (void)n_in; (void)out_size; (void)ws_size;
  Params p;
  p.x = d_in[0]; p.k = d_in[1]; p.v = d_in[2]; p.wq = d_in[3]; p.wo = d_in[4];
  p.wg = d_in[6]; p.wu = d_in[7]; p.wd = d_in[8]; p.c = d_in[9]; p.wm = d_in[10];
  p.out = d_out; p.ws = d_ws;
  hipLaunchKernelGGL(adaln_block_kernel, dim3(NCU), dim3(NTHR), 0, stream, p);
}
```

```cpp
#include <hip/hip_runtime.h>
#include <hip/hip_bf16.h>
#include <stdint.h>

#define GAS __attribute__((address_space(1)))
#define LAS __attribute__((address_space(3)))
typedef unsigned v4u __attribute__((ext_vector_type(4)));
typedef unsigned v2u __attribute__((ext_vector_type(2)));
typedef float f32x4 __attribute__((ext_vector_type(4)));
typedef short s16x4 __attribute__((ext_vector_type(4)));
typedef __bf16 bf16x8 __attribute__((ext_vector_type(8)));
typedef __bf16 bf16x2 __attribute__((ext_vector_type(2)));
typedef LAS unsigned char* lptr;

#define PACE 5
#define FIRST 1
#define PACE_H PACE
#define LATE_WD 0
#define PA PACE
#define PB PACE
#define PRETOUCH 1
#define W_GATE 1
#define W_AT 3
#define EARLY_V2 0
#define STAMPS 0

namespace {
constexpr int NCU = 256, NTHR = 512;
constexpr unsigned KV_BYTES = 4u * 8u * 8192u * 128u * 2u;
constexpr unsigned WS_EPOCH = 0, WS_READY = 4096, WS_H = 8192, WS_MP = 16384,
                   WS_QP = WS_MP + 48 * 4 * 2048,
                   WS_GATE = WS_QP + 8 * 4 * 4 * 2048,
                   WS_STAT = WS_GATE + 16384, WS_ATT = WS_STAT + 8192,
                   WS_PART = WS_ATT + 8 * 32768,
                   WS_DOWN = WS_PART + 256 * 1024, WS_END = WS_DOWN + 56 * 8192, WS_STAMP = WS_END + 65536;
constexpr int RS = 1088;
constexpr int L_WM = 0, L_WQ = 16384, L_WD = 0, L_W = 32768, L_MRG = 98304, L_GATE = L_MRG + 18432, L_ACT = L_GATE + 16384, L_ACTH = L_ACT + 4352, L_X = L_ACTH + 4352,
              L_Q = L_X + 4096, L_RED = L_Q + 1024, L_MISC = L_RED + 8192, L_BYTES = L_MISC + 1024;
constexpr int MRG_H = 576, MRG_W = 4 * MRG_H;
constexpr int M_LN = L_MISC, M_MX = L_MISC + 128, M_LS = L_MISC + 256, M_HID = L_MISC + 384, M_FLAG = L_MISC + 640, M_CLR = L_MISC + 644, M_CNT = L_MISC + 648, M_CN2 = L_MISC + 652, M_FW = L_MISC + 768, M_ZERO = L_MISC + 896;
constexpr unsigned SENT = 0xFFFFFFFFu;
constexpr unsigned SPIN_MAX = 1u << 15;
constexpr int AUX_NT = 2, AUX_SC1 = 16;
constexpr float LOG2E = 1.4426950408889634f;
constexpr int J_G = 8, J_H = 24, J_GR_END = 28;
constexpr int J_B = 8, J_C = 16, J_F_END = 20, J_C_END = 23;

struct Params { const void* x; const void* k; const void* v; const void* wq; const void* wo; const void* wg; const void* wu; const void* wd; const void* c; const void* wm; void* out; void* ws; };

__device__ __forceinline__ float bf_lo(unsigned u) { return __builtin_bit_cast(float, u << 16); }
__device__ __forceinline__ float bf_hi(unsigned u) { return __builtin_bit_cast(float, u & 0xffff0000u); }
__device__ __forceinline__ unsigned pk2(float a, float b) { bf16x2 r = {(__bf16)a, (__bf16)b}; return __builtin_bit_cast(unsigned, r); }
__device__ __forceinline__ float rb(float a) { return bf_lo(pk2(a, a)); }
template <int CTRL> __device__ __forceinline__ float dpp(float x) { return __builtin_bit_cast(float, __builtin_amdgcn_mov_dpp(__builtin_bit_cast(int, x), CTRL, 0xf, 0xf, true)); }
template <int CTRL> __device__ __forceinline__ unsigned dppu(unsigned x) { return (unsigned)__builtin_amdgcn_mov_dpp((int)x, CTRL, 0xf, 0xf, true); }
constexpr int XOR1 = 0xB1, XOR2 = 0x4E, HMIR = 0x141, ROR4 = 0x124, ROR8 = 0x128;
__device__ __forceinline__ float x16_sum(float x) { auto s = __builtin_amdgcn_permlane16_swap(__float_as_uint(x), __float_as_uint(x), false, false); return __uint_as_float(s[0]) + __uint_as_float(s[1]); }
__device__ __forceinline__ float x32_sum(float x) { auto s = __builtin_amdgcn_permlane32_swap(__float_as_uint(x), __float_as_uint(x), false, false); return __uint_as_float(s[0]) + __uint_as_float(s[1]); }
__device__ __forceinline__ float x16_max(float x) { auto s = __builtin_amdgcn_permlane16_swap(__float_as_uint(x), __float_as_uint(x), false, false); return fmaxf(__uint_as_float(s[0]), __uint_as_float(s[1])); }
__device__ __forceinline__ float x32_max(float x) { auto s = __builtin_amdgcn_permlane32_swap(__float_as_uint(x), __float_as_uint(x), false, false); return fmaxf(__uint_as_float(s[0]), __uint_as_float(s[1])); }
__device__ __forceinline__ float row_sum(float x) { x += dpp<XOR1>(x); x += dpp<XOR2>(x); x += dpp<HMIR>(x); x += dpp<ROR8>(x); return x; }
__device__ __forceinline__ float wave_sum(float x) { return x32_sum(x16_sum(row_sum(x))); }
__device__ __forceinline__ void wave_sum2(float& x, float& y) {
  x += dpp<XOR1>(x); y += dpp<XOR1>(y); x += dpp<XOR2>(x); y += dpp<XOR2>(y); x += dpp<HMIR>(x); y += dpp<HMIR>(y); x += dpp<ROR8>(x); y += dpp<ROR8>(y);
  x = x16_sum(x); y = x16_sum(y); x = x32_sum(x); y = x32_sum(y);
}
__device__ __forceinline__ f32x4 mfma(v4u a, v4u b, f32x4 c) { return __builtin_amdgcn_mfma_f32_16x16x32_bf16(__builtin_bit_cast(bf16x8, a), __builtin_bit_cast(bf16x8, b), c, 0, 0, 0); }
__device__ __forceinline__ v2u tr_read(lptr p) { s16x4 r = __builtin_amdgcn_ds_read_tr16_b64_v4i16((LAS s16x4*)p); return __builtin_bit_cast(v2u, r); }
__device__ __forceinline__ v4u tr_frag(lptr p0, lptr p1) { v2u a = tr_read(p0), b = tr_read(p1); return v4u{a.x, a.y, b.x, b.y}; }
__device__ __forceinline__ v4u lds128(lptr p) { return *(LAS v4u*)p; }
__device__ __forceinline__ void bar() { asm volatile("s_waitcnt lgkmcnt(0)\n\ts_barrier" ::: "memory"); }
__device__ __forceinline__ void glds16(const void* gsrc, unsigned lds_dst) {
  unsigned keep;
  lds_dst = __builtin_amdgcn_readfirstlane(lds_dst);
  asm volatile("s_mov_b32 %0, m0\n\ts_mov_b32 m0, %2\n\ts_nop 0\n\tglobal_load_lds_dwordx4 %1, off nt\n\ts_mov_b32 m0, %0" : "=&s"(keep) : "v"(gsrc), "s"(lds_dst) : "memory");
}
__device__ __forceinline__ unsigned nosent(float f) { return min(__float_as_uint(f), SENT - 1u); }
__device__ __forceinline__ unsigned nosentu(unsigned u) { return min(u, SENT - 1u); }
__device__ __forceinline__ unsigned umax4(v4u a) { return max(max(a.x, a.y), max(a.z, a.w)); }
__device__ __forceinline__ bool all_lanes(bool ok) { return __builtin_amdgcn_ballot_w64(!ok) == 0ull; }
__device__ __forceinline__ float sel4(f32x4 d, int g) { const bool o = (g & 1) != 0; const float a = o ? d[1] : d[0], b = o ? d[3] : d[2]; return (g & 2) ? b : a; }
__device__ __forceinline__ bool isnan_(float x) { return x != x; }
#define PIN(x) asm volatile("" : "+v"(x))
__device__ __forceinline__ float fast_rcp(float x) { return __builtin_amdgcn_rcpf(x); }
#define RSRC(p, n) __builtin_amdgcn_make_buffer_rsrc((void*)(p), 0, (int)(n), 0x00020000)
#define LD_SC1(off) __builtin_amdgcn_raw_buffer_load_b128(rws, (off), 0, AUX_SC1)
#define ST_SC1(v, off) __builtin_amdgcn_raw_buffer_store_b128((v), rws, (off), 0, AUX_SC1)
#define FENCE() __builtin_amdgcn_sched_barrier(0)
#define LD8_SC1(off) __builtin_amdgcn_raw_buffer_load_b64(rws, (off), 0, AUX_SC1)
#define GATHER8(N, V, OFF) do { \
    unsigned pend_ = 0; \
    _Pragma("unroll") for (int i = 0; i < (N); ++i) V[i] = LD8_SC1(OFF); \
    _Pragma("unroll") for (int i = 0; i < (N); ++i) pend_ |= all_lanes(max(V[i].x, V[i].y) != SENT) ? 0u : 1u << i; \
    for (unsigned spin_ = 0; pend_ != 0u && spin_ < SPIN_MAX; ++spin_) { \
      asm volatile("" ::: "memory"); \
      _Pragma("unroll") for (int i = 0; i < (N); ++i) if (pend_ & (1u << i)) V[i] = LD8_SC1(OFF); \
      _Pragma("unroll") for (int i = 0; i < (N); ++i) if (pend_ & (1u << i)) { if (all_lanes(max(V[i].x, V[i].y) != SENT)) pend_ &= ~(1u << i); } \
    } } while (0)
#define GATHER(N, V, OFF) do { \
    unsigned pend_ = 0; \
    _Pragma("unroll") for (int i = 0; i < (N); ++i) V[i] = LD_SC1(OFF); \
    _Pragma("unroll") for (int i = 0; i < (N); ++i) pend_ |= all_lanes(umax4(V[i]) != SENT) ? 0u : 1u << i; \
    for (unsigned spin_ = 0; pend_ != 0u && spin_ < SPIN_MAX; ++spin_) { \
      asm volatile("" ::: "memory"); \
      _Pragma("unroll") for (int i = 0; i < (N); ++i) if (pend_ & (1u << i)) V[i] = LD_SC1(OFF); \
      _Pragma("unroll") for (int i = 0; i < (N); ++i) if (pend_ & (1u << i)) { if (all_lanes(umax4(V[i]) != SENT)) pend_ &= ~(1u << i); } \
    } } while (0)
#define STAMP(i) do { } while (0)
#define DUMP() do { } while (0)

__device__ __forceinline__ int img_chunk(int lane, int row) { return ((((lane & 7) >> 1) ^ ((row >> 1) & 3)) << 1) | (lane & 1); }
__device__ __forceinline__ int frag_base(int lane, int ct) { const int g = lane >> 4, q = (lane >> 2) & 3; return (4 * g + q) * 128 + ((ct ^ ((2 * g + (q >> 1)) & 3)) * 32) + (lane & 3) * 8; }
__device__ __forceinline__ v4u w_frag(lptr base, int s) { return tr_frag(base + s * 4096, base + s * 4096 + 2048); }
__device__ __forceinline__ int act_off(int k) { return (k >> 5) * 64 + ((k >> 2) & 3) * 16 + ((k >> 4) & 1) * 8; }
__device__ __forceinline__ void ln_consts(float sum, float sumsq, float& mean_r, float& rden) {
  const float mean = sum * (1.f / 512.f);
  const float var = fmaxf(sumsq * (1.f / 512.f) - mean * mean, 0.f);
  mean_r = rb(mean);
  rden = fast_rcp(rb(__builtin_amdgcn_sqrtf(rb(rb(var) + 1e-5f))));
}
__device__ __forceinline__ float ln_mod(float x, float mean_r, float rden, float c1, float ss) {
  return rb(rb(rb(rb(x - mean_r) * rden) * c1) + ss);
}

__global__ void __launch_bounds__(NTHR, 2) adaln_block_kernel(Params p) {
  __shared__ __attribute__((aligned(16))) unsigned char smem[L_BYTES + (STAMPS ? 1024 : 0)];
  lptr L = (lptr)smem;
  const unsigned lds0 = (unsigned)(uintptr_t)L;
  const int tid = threadIdx.x, lane = tid & 63, w = __builtin_amdgcn_readfirstlane(tid >> 6);
  const int cu = blockIdx.x, X = cu & 7, J = cu >> 3;
  const int g = lane >> 4, n = lane & 15;
  const auto rws = RSRC(p.ws, WS_END);
  const auto rk = RSRC(p.k, KV_BYTES);
  const auto rv = RSRC(p.v, KV_BYTES);
  const bool roleM = J < J_H, roleH = J >= J_H, roleGR = J >= J_H && J < J_GR_END, roleB = J >= J_B && J < J_C, roleC = J >= J_C && J < J_C_END;
  const int kq = J & 3;
  STAMP(0);
  const int mg = J < J_G ? 2 * X + (J >> 2) : 16 + 4 * X + ((J - J_G) >> 2);
  {
    const int r0 = 16 * w + (lane >> 3), ch = img_chunk(lane, r0);
    if (roleM) {
      const GAS unsigned char* s = (const GAS unsigned char*)p.wm + ((size_t)(128 * kq + r0) * 3072 + 64 * mg + 8 * ch) * 2;
      glds16((const void*)s, lds0 + L_WM + w * 2048);
      glds16((const void*)(s + 8 * 3072 * 2), lds0 + L_WM + w * 2048 + 1024);
    }
    const GAS unsigned char* s = (const GAS unsigned char*)p.wq + ((size_t)(128 * kq + r0) * 4096 + 64 * (8 * X + (J >> 2)) + 8 * ch) * 2;
    glds16((const void*)s, lds0 + L_WQ + w * 2048);
    glds16((const void*)(s + 8 * 4096 * 2), lds0 + L_WQ + w * 2048 + 1024);
  }
  const unsigned long long ep = __hip_atomic_load((GAS unsigned long long*)p.ws, __ATOMIC_RELAXED, __HIP_MEMORY_SCOPE_AGENT);
  v2u cv = {0u, 0u};
  if (roleM && tid < 128) cv = *(const GAS v2u*)((const GAS unsigned char*)p.c + ((tid >> 5) * 512 + 128 * kq + 4 * (tid & 31)) * 2);
  asm volatile("s_barrier" ::: "memory");
  if (w == 1) {
    const v4u sent = {SENT, SENT, SENT, SENT};
    ST_SC1(sent, WS_QP + ((X * 4 + g) * 4 + kq) * 2048 + (J >> 2) * 256 + n * 16);
    ST_SC1(sent, WS_PART + cu * 1024 + lane * 16);
    if (lane < 2) ST_SC1(sent, WS_STAT + cu * 32 + lane * 16);
    if (roleB) {
#pragma unroll
      for (int e = 0; e < 4; ++e) ST_SC1(sent, WS_ATT + (2 * e + (lane >> 5)) * 32768 + ((X * 4 + ((lane >> 3) & 3)) * 512 + 64 * (J - J_B)) * 2 + (lane & 7) * 16);
    }
    if (roleGR && n < 8) ST_SC1(sent, WS_GATE + g * 4096 + (4 * X + (J - J_H)) * 128 + n * 16);
    if (roleC) {
#pragma unroll
      for (int e = 0; e < 8; ++e) ST_SC1(sent, WS_DOWN + (7 * X + (J - J_C)) * 8192 + e * 1024 + lane * 16);
    }
  }
  FENCE();
  const bool roleT = roleB || roleC;
  const unsigned kvbase = (((unsigned)(J >> 3) * 8u + (unsigned)X) * 8192u + (unsigned)(J & 7) * 1024u) * 256u + (unsigned)w * 4096u;
  const unsigned koff = (4 * (lane & 3) + ((lane >> 2) & 1)) * 256 + (lane >> 3) * 16;
  v4u kf[8][4];
#define K_ISSUE(t) do { unsigned ko_ = koff; asm volatile("" : "+v"(ko_)); _Pragma("unroll") for (int q_ = 0; q_ < 4; ++q_) kf[t][q_] = __builtin_amdgcn_raw_buffer_load_b128(rk, ko_ + (q_ >> 1) * 512 + (q_ & 1) * 128, kvbase + (t) * 32768, AUX_NT); FENCE(); } while (0)
  K_ISSUE(0);
  if (FIRST > 1) K_ISSUE(1);
  if (w == 2 && PRETOUCH) {
    const GAS unsigned char* a;
    const int i = lane & 7;
    if (roleB) a = (const GAS unsigned char*)p.wo + (size_t)(512 * X + 64 * i) * 1024;
    else if (roleC) a = i < 3 ? (const GAS unsigned char*)p.wg + (size_t)i * 900000 : i < 6 ? (const GAS unsigned char*)p.wu + (size_t)(i - 3) * 900000 : (const GAS unsigned char*)p.wd + (size_t)(32 * (7 * X + (J - J_C))) * 1024;
    else a = (const GAS unsigned char*)p.x;
    glds16((const void*)(a + (lane >> 3) * 0), lds0 + L_RED + 4096);
  }
  if (tid == 0) { *(LAS unsigned*)(L + M_FLAG) = 0u; *(LAS unsigned*)(L + M_CLR) = 0u; *(LAS unsigned*)(L + M_CNT) = 0u; *(LAS unsigned*)(L + M_CN2) = 0u; }
  const unsigned long long E = ep + 1ull;
  const unsigned ta = __builtin_amdgcn_readfirstlane((unsigned)E), tb2 = __builtin_amdgcn_readfirstlane(((unsigned)E * 0x9E3779B1u) ^ (unsigned)(E >> 32));
  if (roleM && tid < 128) {
    float cs[4] = {bf_lo(cv.x), bf_hi(cv.x), bf_lo(cv.y), bf_hi(cv.y)};
#pragma unroll
    for (int e = 0; e < 4; ++e) cs[e] = cs[e] / (1.f + expf(-cs[e]));
    *(LAS v2u*)(L + L_ACT + (tid >> 5) * RS + act_off(4 * (tid & 31))) = v2u{pk2(cs[0], cs[1]), pk2(cs[2], cs[3])};
  }
  STAMP(17);
  asm volatile("s_waitcnt vmcnt(%0)" ::"i"(4 * FIRST) : "memory");
  STAMP(18);
  if (w == 1) {
    if (lane == 0) ST_SC1((v4u{0u, ta, 0u, tb2}), WS_READY + cu * 16);
  }
  bar();
  if (w == 1 && lane == 0) *(volatile LAS unsigned*)(L + M_CLR) = 1u;
  STAMP(1);
  if (roleM && w < 4) {
    lptr ap = L + L_ACT + (lane & 3) * RS + g * 16;
    lptr wp = L + L_WM + frag_base(lane, w);
    f32x4 a0 = mfma(lds128(ap), w_frag(wp, 0), f32x4{0.f, 0.f, 0.f, 0.f}), a1 = mfma(lds128(ap + 64), w_frag(wp, 1), f32x4{0.f, 0.f, 0.f, 0.f});
    a0 = mfma(lds128(ap + 128), w_frag(wp, 2), a0); a1 = mfma(lds128(ap + 192), w_frag(wp, 3), a1);
    const float v = sel4(a0 + a1, g);
    const float vn = dpp<XOR1>(v);
    if ((n & 1) == 0) ST_SC1((v4u{__float_as_uint(v), ta, __float_as_uint(vn), tb2}), WS_MP + ((mg * 4 + kq) * 4 + g) * 512 + (8 * w + (n >> 1)) * 16);
    FENCE();
  }
  if (roleH && w == 0) {
    __builtin_amdgcn_s_setprio(2);
    const int eta = 8 * X + (J - J_H), b = (lane >> 2) & 3, pr = lane & 3;
    v4u xq[4];
#pragma unroll
    for (int i = 0; i < 4; ++i) xq[i] = *(const GAS v4u*)((const GAS unsigned char*)p.x + (g * 512 + 32 * n + 8 * i) * 2);
    const unsigned xp = *(const GAS unsigned*)((const GAS unsigned char*)p.x + (b * 512 + 8 * eta + 2 * pr) * 2);
    float s1 = 0.f, s2 = 0.f;
#pragma unroll
    for (int i = 0; i < 4; ++i)
#pragma unroll
      for (int e = 0; e < 4; ++e) { const float a = bf_lo(xq[i][e]), c = bf_hi(xq[i][e]); s1 += a + c; s2 += a * a + c * c; }
    s1 = row_sum(s1); s2 = row_sum(s2);
    float mean_r, rden;
    ln_consts(s1, s2, mean_r, rden);
    mean_r = __builtin_bit_cast(float, __builtin_amdgcn_ds_bpermute(64 * b, __builtin_bit_cast(int, mean_r)));
    rden = __builtin_bit_cast(float, __builtin_amdgcn_ds_bpermute(64 * b, __builtin_bit_cast(int, rden)));
    v4u ms, mc;
    for (unsigned spin = 0; spin < SPIN_MAX; ++spin) {
      ms = LD_SC1(WS_MP + (((eta >> 3) * 4 + g) * 4 + b) * 512 + ((eta & 7) * 4 + pr) * 16);
      mc = LD_SC1(WS_MP + (((8 + (eta >> 3)) * 4 + g) * 4 + b) * 512 + ((eta & 7) * 4 + pr) * 16);
      if (all_lanes((ms.y == ta) & (ms.w == tb2) & (mc.y == ta) & (mc.w == tb2))) break;
      asm volatile("" ::: "memory");
    }
    const float sa0 = rb(x32_sum(x16_sum(__uint_as_float(ms.x)))), sa1 = rb(x32_sum(x16_sum(__uint_as_float(ms.z))));
    const float ca0 = rb(x32_sum(x16_sum(__uint_as_float(mc.x)))), ca1 = rb(x32_sum(x16_sum(__uint_as_float(mc.z))));
    const unsigned hp = pk2(ln_mod(bf_lo(xp), mean_r, rden, rb(1.f + ca0), sa0), ln_mod(bf_hi(xp), mean_r, rden, rb(1.f + ca1), sa1));
    const unsigned hn = dppu<XOR1>(hp);
    if (lane < 16 && (pr & 1) == 0) ST_SC1((v4u{hp, ta, hn, tb2}), WS_H + eta * 128 + (b * 2 + (pr >> 1)) * 16);
    __builtin_amdgcn_s_setprio(0);
    FENCE();
  }
  STAMP(2);
#define PACE_WAITN(x) do { if (w != 0 && PACE >= 0) asm volatile("s_waitcnt vmcnt(%0)" ::"i"(PACE < 0 ? 0 : PACE + (x)) : "memory"); } while (0)
#define PACE_A() do { if (w != 0) asm volatile("s_waitcnt vmcnt(%0)" ::"i"(PA) : "memory"); } while (0)
#define PACE_B() do { if (w != 0) asm volatile("s_waitcnt vmcnt(%0)" ::"i"(PB) : "memory"); } while (0)
#define PACE_WAIT2() PACE_WAITN(4)
#define PACE_WAIT() PACE_WAITN(0)
  if (FIRST < 2) K_ISSUE(1);

  PACE_A(); K_ISSUE(2); PACE_A(); K_ISSUE(3);
  if (w == 0) {
    __builtin_amdgcn_s_setprio(2);
    v4u bq[4][4];
#pragma unroll
    for (int ct = 0; ct < 4; ++ct) { lptr wp = L + L_WQ + frag_base(lane, ct);
#pragma unroll
      for (int s = 0; s < 4; ++s) bq[ct][s] = w_frag(wp, s); }
    v4u hv[2];
    for (unsigned spin = 0; spin < SPIN_MAX; ++spin) {
      bool ok = true;
#pragma unroll
      for (int i = 0; i < 2; ++i) { hv[i] = LD_SC1(WS_H + (128 * kq + 64 * i + lane) * 16); ok &= (hv[i].y == ta) & (hv[i].w == tb2); }
#pragma unroll
      for (int i = 0; i < 4; ++i) { const v4u rd = LD_SC1(WS_READY + (64 * i + lane) * 16); ok &= (rd.y == ta) & (rd.w == tb2); }
      if (all_lanes(ok)) break;
      asm volatile("" ::: "memory");
    }
    STAMP(3);
#pragma unroll
    for (int i = 0; i < 2; ++i) { const int gm = 64 * i + lane; *(LAS v2u*)(L + L_ACTH + ((gm >> 1) & 3) * RS + act_off(8 * (gm >> 3) + 4 * (gm & 1))) = v2u{hv[i].x, hv[i].z}; }
    asm volatile("s_waitcnt lgkmcnt(0)" ::: "memory");
    lptr ap = L + L_ACTH + (lane & 3) * RS + g * 16;
    const v4u a0 = lds128(ap), a1 = lds128(ap + 64), a2 = lds128(ap + 128), a3 = lds128(ap + 192);
    float qv[4];
#pragma unroll
    for (int ct = 0; ct < 4; ++ct) {
      const f32x4 z = {0.f, 0.f, 0.f, 0.f};
      qv[ct] = sel4(mfma(a1, bq[ct][1], mfma(a0, bq[ct][0], z)) + mfma(a3, bq[ct][3], mfma(a2, bq[ct][2], z)), g);
    }
    while (*(volatile LAS unsigned*)(L + M_CLR) == 0u) __builtin_amdgcn_s_sleep(1);
    ST_SC1((v4u{nosent(qv[0]), nosent(qv[1]), nosent(qv[2]), nosent(qv[3])}), WS_QP + ((X * 4 + g) * 4 + kq) * 2048 + (J >> 2) * 256 + n * 16);
    if (cu == 0 && lane == 0) __hip_atomic_store((GAS unsigned long long*)p.ws, E, __ATOMIC_RELAXED, __HIP_MEMORY_SCOPE_AGENT);
    __builtin_amdgcn_s_setprio(0);
    FENCE();
    STAMP(4);
  }
  PACE_B(); K_ISSUE(4); PACE_B(); K_ISSUE(5);
  PACE_WAIT(); K_ISSUE(6); PACE_WAIT(); K_ISSUE(7);
  if (w == 0) {
    __builtin_amdgcn_s_setprio(2);
    v4u qp[8];
    for (unsigned spin = 0; spin < SPIN_MAX; ++spin) {
      unsigned m = 0;
#pragma unroll
      for (int i = 0; i < 8; ++i) { qp[i] = LD_SC1(WS_QP + ((X * 4 + (J >> 3)) * 4 + (i >> 1)) * 2048 + ((i & 1) * 64 + lane) * 16); m = max(m, umax4(qp[i])); }
      if (all_lanes(m != SENT)) break;
      asm volatile("" ::: "memory");
    }
#pragma unroll
    for (int hf = 0; hf < 2; ++hf)
#pragma unroll
      for (int ct = 0; ct < 4; ++ct) {
        const float s = (__uint_as_float(qp[hf][ct]) + __uint_as_float(qp[2 + hf][ct])) + (__uint_as_float(qp[4 + hf][ct]) + __uint_as_float(qp[6 + hf][ct]));
        *(LAS unsigned short*)(L + L_Q + (64 * (4 * hf + g) + 16 * ct + n) * 2) = (unsigned short)(pk2(s, s) & 0xffffu);
      }
    asm volatile("s_waitcnt lgkmcnt(0)" ::: "memory");
    STAMP(5);
    if (roleGR) {
      const int gg = 4 * X + (J - J_H);
      v4u gv[8];
      for (unsigned spin = 0; spin < SPIN_MAX; ++spin) {
        bool ok = true;
#pragma unroll
        for (int i = 0; i < 8; ++i) { gv[i] = LD_SC1(WS_MP + (((16 + gg) * 4 + (i >> 1)) * 4 + g) * 512 + ((i & 1) * 16 + n) * 16); ok &= (gv[i].y == ta) & (gv[i].w == tb2); }
        if (all_lanes(ok)) break;
        asm volatile("" ::: "memory");
      }
#pragma unroll
      for (int hf = 0; hf < 2; ++hf) {
        const float s0 = (__uint_as_float(gv[hf].x) + __uint_as_float(gv[2 + hf].x)) + (__uint_as_float(gv[4 + hf].x) + __uint_as_float(gv[6 + hf].x));
        const float s1 = (__uint_as_float(gv[hf].z) + __uint_as_float(gv[2 + hf].z)) + (__uint_as_float(gv[4 + hf].z) + __uint_as_float(gv[6 + hf].z));
        __builtin_amdgcn_raw_buffer_store_b32(nosentu(pk2(s0, s1)), rws, WS_GATE + g * 4096 + gg * 128 + (16 * hf + n) * 4, 0, AUX_SC1);
      }
    }
    __builtin_amdgcn_s_setprio(0);
    FENCE();
  }
  v4u vf[4][8];
#define V_ISSUE(u) do { unsigned vo_ = lane * 16; asm volatile("" : "+v"(vo_)); _Pragma("unroll") for (int j_ = 0; j_ < 8; ++j_) vf[u][j_] = __builtin_amdgcn_raw_buffer_load_b128(rv, vo_ + (j_ & 3) * 1024, kvbase + (2 * (u) + (j_ >> 2)) * 32768, AUX_NT); FENCE(); } while (0)
  PACE_WAIT(); V_ISSUE(0);
  PACE_WAIT2(); V_ISSUE(1);
  STAMP(6);
  bar();
  STAMP(7);
  const v4u qf0 = lds128(L + L_Q + (n & 3) * 256 + g * 32 + ((n >> 2) & 1) * 16), qf1 = lds128(L + L_Q + (n & 3) * 256 + 128 + g * 32 + ((n >> 2) & 1) * 16);
  float S[8][4];
#define QK(t) do { const f32x4 z_ = {0.f, 0.f, 0.f, 0.f}; \
    const f32x4 d0_ = mfma(kf[t][1], qf1, mfma(kf[t][0], qf0, z_)), d1_ = mfma(kf[t][3], qf1, mfma(kf[t][2], qf0, z_)); \
    _Pragma("unroll") for (int r_ = 0; r_ < 4; ++r_) { \
      auto s_ = __builtin_amdgcn_permlane32_swap(__float_as_uint(d0_[r_]), __float_as_uint(d1_[r_]), false, false); \
      S[t][r_] = __uint_as_float(s_[0]) + dpp<ROR4>(__uint_as_float(s_[1])); } } while (0)
  QK(0); QK(1); FENCE(); V_ISSUE(2);
  QK(2); QK(3); FENCE(); V_ISSUE(3);
  if (W_GATE && lane == 0) (void)__hip_atomic_fetch_add((LAS unsigned*)(L + M_CN2), 1u, __ATOMIC_RELAXED, __HIP_MEMORY_SCOPE_WORKGROUP);
  QK(4); QK(5); QK(6); QK(7); FENCE();
  STAMP(8);
  unsigned S2[8][2];
  float mx = -__builtin_inff();
#pragma unroll
  for (int t = 0; t < 8; ++t)
#pragma unroll
    for (int r = 0; r < 2; ++r) {
      const unsigned a = pk2(S[t][2 * r], S[t][2 * r + 1]);
      S2[t][r] = pk2(bf_lo(a) * 0.08838834764831845f, bf_hi(a) * 0.08838834764831845f);
      mx = fmaxf(mx, fmaxf(bf_lo(S2[t][r]), bf_hi(S2[t][r])));
    }
  FENCE();
  FENCE();
  mx = x32_max(x16_max(mx));
  float ls = 0.f;
  v4u pf[4];
#pragma unroll
  for (int u = 0; u < 4; ++u) {
    float e[8];
#pragma unroll
    for (int j = 0; j < 8; ++j) { const unsigned sp = S2[2 * u + (j >> 2)][(j & 3) >> 1]; e[j] = __builtin_amdgcn_exp2f((((j & 1) ? bf_hi(sp) : bf_lo(sp)) - mx) * LOG2E); ls += e[j]; }
    pf[u] = v4u{pk2(e[0], e[1]), pk2(e[2], e[3]), pk2(e[4], e[5]), pk2(e[6], e[7])};
  }
  ls = x32_sum(x16_sum(ls));
  if (lane < 4) { *(LAS float*)(L + M_MX + lane * 32 + w * 4) = mx; *(LAS float*)(L + M_LS + lane * 32 + w * 4) = ls; }
  {
    asm volatile("s_waitcnt lgkmcnt(0)" ::: "memory");
    unsigned old = 0;
    if (lane == 0) old = __hip_atomic_fetch_add((LAS unsigned*)(L + M_CNT), 1u, __ATOMIC_RELAXED, __HIP_MEMORY_SCOPE_WORKGROUP);
    if (__builtin_amdgcn_readfirstlane(old) == 7u) {
      const float mw = *(volatile LAS float*)(L + M_MX + ((lane >> 3) & 3) * 32 + (lane & 7) * 4), lw = *(volatile LAS float*)(L + M_LS + ((lane >> 3) & 3) * 32 + (lane & 7) * 4);
      float M = fmaxf(mw, dpp<XOR1>(mw)); M = fmaxf(M, dpp<XOR2>(M)); M = fmaxf(M, dpp<HMIR>(M));
      const float f = __builtin_amdgcn_exp2f((mw - M) * LOG2E);
      float Lc = f * lw; Lc += dpp<XOR1>(Lc); Lc += dpp<XOR2>(Lc); Lc += dpp<HMIR>(Lc);
      if (lane < 32) *(LAS float*)(L + M_FW + lane * 4) = f;
      if (lane < 32 && (lane & 7) == 0) __builtin_amdgcn_raw_buffer_store_b64((v2u{nosent(M), nosent(Lc)}), rws, WS_STAT + cu * 32 + (lane >> 3) * 8, 0, AUX_SC1);
    }
  }
  FENCE();
  STAMP(9);
  v4u wr[8];
#define W_LOADS() do { \
     \
    const int pc_ = 7 * X + (J - J_C); \
    const auto rwa = RSRC(roleB ? p.wo : p.wg, roleB ? 4096 * 512 * 2 : roleC ? 512 * 1792 * 2 : 0); \
    const auto rwb = RSRC(roleB ? p.wo : p.wu, roleB ? 4096 * 512 * 2 : roleC ? 512 * 1792 * 2 : 0); \
    const int rB = 64 * w + (lane >> 3); \
    const int rS = 128 * (w >> 1) + 32 * ((lane >> 5) & 1) + 8 * ((lane >> 3) & 3) + 4 * (w & 1); \
    const unsigned vo = roleB ? ((512 * X + rS) * 512 + 64 * (J - J_B) + 8 * img_chunk(lane, rB)) * 2 : ((64 * w + (lane >> 2)) * 1792 + 32 * pc_ + 8 * (lane & 3)) * 2; \
_Pragma("unroll") \
    for (int e = 0; e < 4; ++e) wr[e] = __builtin_amdgcn_raw_buffer_load_b128(rwa, vo, roleB ? (64 * (e & 1) + (e >> 1)) * 1024 : e * 16 * 1792 * 2, AUX_NT); \
_Pragma("unroll") \
    for (int e = 0; e < 4; ++e) wr[4 + e] = __builtin_amdgcn_raw_buffer_load_b128(rwb, vo, roleB ? (64 * (e & 1) + 2 + (e >> 1)) * 1024 : e * 16 * 1792 * 2, AUX_NT); \
  } while (0)
  f32x4 oa[8];
#pragma unroll
  for (int d = 0; d < 8; ++d) oa[d] = f32x4{0.f, 0.f, 0.f, 0.f};
#define PV(u) do { _Pragma("unroll") for (int d = 0; d < 8; ++d) { \
      const unsigned sel = (d & 1) ? 0x07060302u : 0x05040100u; v4u a; \
      _Pragma("unroll") for (int i = 0; i < 4; ++i) a[i] = __builtin_amdgcn_perm(vf[u][2 * i + 1][d >> 1], vf[u][2 * i][d >> 1], sel); \
      oa[d] = mfma(a, pf[u], oa[d]); } FENCE(); } while (0)
  PV(0); PV(1);
  if (W_AT == 1) W_LOADS();
  PV(2);
  if (W_AT == 2) W_LOADS();
  PV(3);
  if (W_AT == 3) {
    if (W_GATE && roleT) { for (unsigned spin = 0; spin < (SPIN_MAX << 4); ++spin) { if (*(volatile LAS unsigned*)(L + M_CN2) >= 8u) break; __builtin_amdgcn_s_sleep(1); } }
    W_LOADS(); FENCE();
  }
  STAMP(10);
  int tid2 = threadIdx.x; asm volatile("" : "+v"(tid2));
#define tid tid2
  const int lane2 = tid2 & 63;
#define lane lane2
  const int g2 = lane2 >> 4, n2 = lane2 & 15;
#define g g2
#define n n2
  const int tb = tid2 >> 7, tk = 4 * (tid2 & 127);
  if (n < 4) {
#pragma unroll
    for (int d = 0; d < 8; ++d) *(LAS f32x4*)(L + L_MRG + w * MRG_W + n * MRG_H + d * 64 + g * 16) = oa[d];
  }
  bar();
  {
    const f32x4 f0 = *(LAS f32x4*)(L + M_FW + tb * 32), f1 = *(LAS f32x4*)(L + M_FW + tb * 32 + 16);
    float o = 0.f;
#pragma unroll
    for (int ww = 0; ww < 8; ++ww) o += (ww < 4 ? f0[ww & 3] : f1[ww & 3]) * *(LAS float*)(L + L_MRG + ww * MRG_W + tb * MRG_H + (tid & 127) * 4);
    const float on = dpp<XOR1>(o);
    if ((tid & 1) == 0) __builtin_amdgcn_raw_buffer_store_b32(nosentu(pk2(o, on)), rws, WS_PART + cu * 1024 + tid * 2, 0, AUX_SC1);
  }
  STAMP(11);
  if (!(roleB || roleC)) { STAMP(16); DUMP(); return; }
  v4u wdr[4], gt[2];
  v2u xr;
  if (roleC) {
#pragma unroll
    for (int e = 0; e < 2; ++e) gt[e] = LD_SC1(WS_GATE + ((2 * w + e) * 64 + lane) * 16);
    xr = *(const GAS v2u*)((const GAS unsigned char*)p.x + tid * 8);
    const auto rd = RSRC(p.wd, 1792 * 512 * 2);
    const unsigned o = ((32 * (7 * X + (J - J_C)) + (lane >> 3)) * 512 + 64 * w + 8 * img_chunk(lane, lane >> 3)) * 2;
#pragma unroll
    for (int e = 0; e < 4; ++e) wdr[e] = __builtin_amdgcn_raw_buffer_load_b128(rd, o, e * 8 * 512 * 2, AUX_NT);
  }
  if (roleB) {
#pragma unroll
    for (int e = 0; e < 8; ++e) *(LAS v4u*)(L + L_W + w * 8192 + e * 1024 + lane * 16) = wr[e];
  } else {
    const int rw = lane >> 2, x = (rw >> 1) & 3, c = lane & 3;
    lptr pg = L + L_W + w * 8192 + rw * 128 + ((((c >> 1) ^ x) << 1) | (c & 1)) * 16, pu = L + L_W + w * 8192 + rw * 128 + ((((2 + (c >> 1)) ^ x) << 1) | (c & 1)) * 16;
#pragma unroll
    for (int e = 0; e < 4; ++e) { *(LAS v4u*)(pg + e * 2048) = wr[e]; *(LAS v4u*)(pu + e * 2048) = wr[4 + e]; }
  }
  if (roleB) {
    const int bb = w >> 1, hf = lane >> 5;
    v2u st[2];
    for (unsigned spin = 0; spin < SPIN_MAX; ++spin) {
#pragma unroll
      for (int hh = 0; hh < 2; ++hh) st[hh] = __builtin_amdgcn_raw_buffer_load_b64(rws, WS_STAT + ((8 * bb + 2 * (lane & 3) + hf) * 8 + X) * 32 + (2 * (w & 1) + hh) * 8, 0, AUX_SC1);
      if (all_lanes(max(max(st[0].x, st[0].y), max(st[1].x, st[1].y)) != SENT)) break;
      asm volatile("" ::: "memory");
    }
    float wt[2][4];
#pragma unroll
    for (int hh = 0; hh < 2; ++hh) {
      const float ms = __uint_as_float(st[hh].x);
      float M = fmaxf(ms, dpp<XOR1>(ms)); M = fmaxf(M, dpp<XOR2>(M)); M = x32_max(M);
      const float f = __builtin_amdgcn_exp2f((ms - M) * LOG2E);
      float Ls = f * __uint_as_float(st[hh].y); Ls += dpp<XOR1>(Ls); Ls += dpp<XOR2>(Ls); Ls = x32_sum(Ls);
      const float x = f * fast_rcp(Ls);
      wt[hh][0] = dpp<0x00>(x); wt[hh][1] = dpp<0x55>(x); wt[hh][2] = dpp<0xAA>(x); wt[hh][3] = dpp<0xFF>(x);
    }
    asm volatile("s_waitcnt lgkmcnt(0)" ::: "memory");
    v4u bo[2][4];
#pragma unroll
    for (int ct = 0; ct < 4; ++ct) { lptr wp = L + L_W + w * 8192 + frag_base(lane, ct); bo[0][ct] = w_frag(wp, 0); bo[1][ct] = w_frag(wp, 1); }
    int o_act = L_ACT + bb * RS + act_off(256 * (w & 1) + 4 * (lane & 31));
    int o_a = L_ACT + (lane & 3) * RS + g * 16 + (2 * w) * 64;
    int o_red = L_RED + w * 1024 + g * 256 + n * 4, o_rd = L_RED + lane * 16;
    int o_src = WS_PART + ((8 * bb + hf) * 8 + X) * 1024 + (w & 1) * 512 + (lane & 31) * 8;
    int o_dst = WS_ATT + w * 32768 + ((X * 4 + g) * 512 + 64 * (J - J_B) + 4 * n) * 2;
    PIN(o_act); PIN(o_a); PIN(o_red); PIN(o_rd); PIN(o_src); PIN(o_dst);
    STAMP(19);
    float o[2][4];
    {
      v2u po[8];
      GATHER8(8, po, o_src + (i & 3) * 2 * 8 * 1024 + (i >> 2) * 256);
#pragma unroll
      for (int hh = 0; hh < 2; ++hh) {
        o[hh][0] = x32_sum(wt[hh][0] * bf_lo(po[4 * hh].x) + wt[hh][1] * bf_lo(po[4 * hh + 1].x) + wt[hh][2] * bf_lo(po[4 * hh + 2].x) + wt[hh][3] * bf_lo(po[4 * hh + 3].x));
        o[hh][1] = x32_sum(wt[hh][0] * bf_hi(po[4 * hh].x) + wt[hh][1] * bf_hi(po[4 * hh + 1].x) + wt[hh][2] * bf_hi(po[4 * hh + 2].x) + wt[hh][3] * bf_hi(po[4 * hh + 3].x));
        o[hh][2] = x32_sum(wt[hh][0] * bf_lo(po[4 * hh].y) + wt[hh][1] * bf_lo(po[4 * hh + 1].y) + wt[hh][2] * bf_lo(po[4 * hh + 2].y) + wt[hh][3] * bf_lo(po[4 * hh + 3].y));
        o[hh][3] = x32_sum(wt[hh][0] * bf_hi(po[4 * hh].y) + wt[hh][1] * bf_hi(po[4 * hh + 1].y) + wt[hh][2] * bf_hi(po[4 * hh + 2].y) + wt[hh][3] * bf_hi(po[4 * hh + 3].y));
      }
    }
    STAMP(12);
    if (lane < 32) {
      *(LAS v2u*)(L + o_act) = v2u{pk2(o[0][0], o[0][1]), pk2(o[0][2], o[0][3])};
      *(LAS v2u*)(L + o_act + 256) = v2u{pk2(o[1][0], o[1][1]), pk2(o[1][2], o[1][3])};
    }
    bar();
    {
      const v4u a0 = lds128(L + o_a), a1 = lds128(L + o_a + 64);
#pragma unroll
      for (int ct = 0; ct < 4; ++ct) {
        f32x4 d = mfma(a0, bo[0][ct], f32x4{0.f, 0.f, 0.f, 0.f});
        d = mfma(a1, bo[1][ct], d);
        *(LAS float*)(L + o_red + ct * 64) = sel4(d, g);
      }
    }
    bar();
    {
      f32x4 s0 = *(LAS f32x4*)(L + o_rd), s1 = *(LAS f32x4*)(L + o_rd + 1024);
#pragma unroll
      for (int ww = 2; ww < 8; ww += 2) { s0 += *(LAS f32x4*)(L + o_rd + ww * 1024); s1 += *(LAS f32x4*)(L + o_rd + ww * 1024 + 1024); }
      s0 += s1;
      __builtin_amdgcn_raw_buffer_store_b64((v2u{nosentu(pk2(s0.x, s0.y)), nosentu(pk2(s0.z, s0.w))}), rws, o_dst, 0, AUX_SC1);
    }
    STAMP(13); STAMP(16); DUMP();
    return;
  }
  const int pc = 7 * X + (J - J_C);
#pragma unroll
  for (int e = 0; e < 4; ++e) *(LAS v4u*)(L + L_WD + w * 4096 + e * 1024 + lane * 16) = wdr[e];
  if (!all_lanes(umax4(gt[0]) != SENT && umax4(gt[1]) != SENT)) {
    for (unsigned spin = 0; spin < SPIN_MAX; ++spin) {
      asm volatile("" ::: "memory");
#pragma unroll
      for (int e = 0; e < 2; ++e) gt[e] = LD_SC1(WS_GATE + ((2 * w + e) * 64 + lane) * 16);
      if (all_lanes(umax4(gt[0]) != SENT && umax4(gt[1]) != SENT)) break;
    }
  }
#pragma unroll
  for (int e = 0; e < 2; ++e) *(LAS v4u*)(L + L_GATE + ((2 * w + e) * 64 + lane) * 16) = gt[e];
  if (tid < 4) *(LAS v4u*)(L + M_ZERO + tid * 16) = v4u{0u, 0u, 0u, 0u};
  bar();
  {
    const v2u ga = *(LAS v2u*)(L + L_GATE + tb * 4096 + tk * 2), sb = *(LAS v2u*)(L + L_GATE + tb * 4096 + 1024 + tk * 2), cb = *(LAS v2u*)(L + L_GATE + tb * 4096 + 2048 + tk * 2);
    const v2u xw = xr;
    const float gav[4] = {bf_lo(ga.x), bf_hi(ga.x), bf_lo(ga.y), bf_hi(ga.y)}, sbv[4] = {bf_lo(sb.x), bf_hi(sb.x), bf_lo(sb.y), bf_hi(sb.y)},
                c1v[4] = {rb(1.f + bf_lo(cb.x)), rb(1.f + bf_hi(cb.x)), rb(1.f + bf_lo(cb.y)), rb(1.f + bf_hi(cb.y))}, xs[4] = {bf_lo(xw.x), bf_hi(xw.x), bf_lo(xw.y), bf_hi(xw.y)};
    v4u bg[2][4], bd[4];
#pragma unroll
    for (int ct = 0; ct < 4; ++ct) { lptr wp = L + L_W + w * 8192 + frag_base(lane, ct); bg[0][ct] = w_frag(wp, 0); bg[1][ct] = w_frag(wp, 1); }
    {
      const int q = (lane >> 2) & 3;
#pragma unroll
      for (int kp = 0; kp < 4; ++kp) {
        const int ra = 8 * kp + q, rb_ = ra + 4;
        bd[kp] = tr_frag(L + L_WD + w * 4096 + ra * 128 + ((g ^ ((ra >> 1) & 3)) * 32) + (lane & 3) * 8, L + L_WD + w * 4096 + rb_ * 128 + ((g ^ ((rb_ >> 1) & 3)) * 32) + (lane & 3) * 8);
      }
    }
    int o_src = WS_ATT + X * 32768 + (tb * 512 + tk) * 2;
    int o_x = L_X + tid * 8, o_ln = M_LN + tb * 16, o_act = L_ACT + tb * RS + act_off(tk), o_a = L_ACT + (lane & 3) * RS + g * 16 + (2 * w) * 64;
    int o_red = L_RED + w * 1024 + g * 256 + n * 4, o_rd = L_RED + (2 * w + (lane >> 5)) * 256 + (lane & 31) * 4;
    int o_hw = M_HID + (2 * w + (lane >> 5)) * 64 + (lane & 31) * 2, o_hr = (n >> 2) == g ? M_HID + (n & 3) * 64 : M_ZERO;
    int o_dst = WS_DOWN + pc * 8192 + tid * 16;
    PIN(o_src); PIN(o_x); PIN(o_ln); PIN(o_act); PIN(o_a); PIN(o_red); PIN(o_rd); PIN(o_hw); PIN(o_hr); PIN(o_dst);
    float gv[4] = {gav[0], gav[1], gav[2], gav[3]}, sv[4] = {sbv[0], sbv[1], sbv[2], sbv[3]}, cv1[4] = {c1v[0], c1v[1], c1v[2], c1v[3]}, xx[4] = {xs[0], xs[1], xs[2], xs[3]};
#pragma unroll
    for (int e = 0; e < 4; ++e) { PIN(gv[e]); PIN(sv[e]); PIN(cv1[e]); PIN(xx[e]); }
    STAMP(19);
    float at[4];
    {
      v2u av[8];
      GATHER8(8, av, o_src + i * 4 * 1024);
      at[0] = ((bf_lo(av[0].x) + bf_lo(av[1].x)) + (bf_lo(av[2].x) + bf_lo(av[3].x))) + ((bf_lo(av[4].x) + bf_lo(av[5].x)) + (bf_lo(av[6].x) + bf_lo(av[7].x)));
      at[1] = ((bf_hi(av[0].x) + bf_hi(av[1].x)) + (bf_hi(av[2].x) + bf_hi(av[3].x))) + ((bf_hi(av[4].x) + bf_hi(av[5].x)) + (bf_hi(av[6].x) + bf_hi(av[7].x)));
      at[2] = ((bf_lo(av[0].y) + bf_lo(av[1].y)) + (bf_lo(av[2].y) + bf_lo(av[3].y))) + ((bf_lo(av[4].y) + bf_lo(av[5].y)) + (bf_lo(av[6].y) + bf_lo(av[7].y)));
      at[3] = ((bf_hi(av[0].y) + bf_hi(av[1].y)) + (bf_hi(av[2].y) + bf_hi(av[3].y))) + ((bf_hi(av[4].y) + bf_hi(av[5].y)) + (bf_hi(av[6].y) + bf_hi(av[7].y)));
    }
    STAMP(14);
    unsigned xp[2];
    float x2[4];
#pragma unroll
    for (int e = 0; e < 4; e += 2) {
      const unsigned a = pk2(at[e], at[e + 1]);
      const unsigned t = pk2(gv[e] * bf_lo(a), gv[e + 1] * bf_hi(a));
      xp[e >> 1] = pk2(xx[e] + bf_lo(t), xx[e + 1] + bf_hi(t));
      x2[e] = bf_lo(xp[e >> 1]); x2[e + 1] = bf_hi(xp[e >> 1]);
    }
    float s1 = (x2[0] + x2[1]) + (x2[2] + x2[3]), s2 = (x2[0] * x2[0] + x2[1] * x2[1]) + (x2[2] * x2[2] + x2[3] * x2[3]);
    wave_sum2(s1, s2);
    if (lane == 0) *(LAS v2u*)(L + M_LN + w * 8) = v2u{__float_as_uint(s1), __float_as_uint(s2)};
    *(LAS v2u*)(L + o_x) = v2u{xp[0], xp[1]};
    bar();
    STAMP(20);
    {
      const f32x4 s = *(LAS f32x4*)(L + o_ln);
      float mean_r, rden;
      ln_consts(s.x + s.z, s.y + s.w, mean_r, rden);
      unsigned hp[2];
#pragma unroll
      for (int e = 0; e < 4; e += 2) {
        const unsigned a = pk2(x2[e] - mean_r, x2[e + 1] - mean_r);
        const unsigned t = pk2(bf_lo(a) * rden, bf_hi(a) * rden);
        const unsigned u = pk2(bf_lo(t) * cv1[e], bf_hi(t) * cv1[e + 1]);
        hp[e >> 1] = pk2(bf_lo(u) + sv[e], bf_hi(u) + sv[e + 1]);
      }
      *(LAS v2u*)(L + o_act) = v2u{hp[0], hp[1]};
    }
    bar();
    STAMP(21);
    {
      const v4u a0 = lds128(L + o_a), a1 = lds128(L + o_a + 64);
#pragma unroll
      for (int ct = 0; ct < 4; ++ct) {
        f32x4 d = mfma(a0, bg[0][ct], f32x4{0.f, 0.f, 0.f, 0.f});
        d = mfma(a1, bg[1][ct], d);
        *(LAS float*)(L + o_red + ct * 64) = sel4(d, g);
      }
    }
    bar();
    STAMP(22);
    if (w < 2) {
      float p0[8], p1[8];
#pragma unroll
      for (int ww = 0; ww < 8; ++ww) { p0[ww] = *(LAS float*)(L + o_rd + ww * 1024); p1[ww] = *(LAS float*)(L + o_rd + ww * 1024 + 128); }
      const unsigned gu = pk2(((p0[0] + p0[1]) + (p0[2] + p0[3])) + ((p0[4] + p0[5]) + (p0[6] + p0[7])), ((p1[0] + p1[1]) + (p1[2] + p1[3])) + ((p1[4] + p1[5]) + (p1[6] + p1[7])));
      const float sg = bf_lo(gu), up = bf_hi(gu);
      const float u = sg * (1.f + 0.044715f * sg * sg) * (-2.f * LOG2E * 0.7978845608028654f);
      const float ge = rb(sg * fast_rcp(1.f + __builtin_amdgcn_exp2f(u)));
      *(LAS unsigned short*)(L + o_hw) = (unsigned short)(pk2(ge * up, 0.f) & 0xffffu);
    }
    bar();
    STAMP(23);
    {
      const v4u h0 = lds128(L + o_hr), h1 = lds128(L + o_hr + 16), h2 = lds128(L + o_hr + 32), h3 = lds128(L + o_hr + 48);
      const f32x4 z = {0.f, 0.f, 0.f, 0.f};
      const f32x4 d = mfma(h1, bd[1], mfma(h0, bd[0], z)) + mfma(h3, bd[3], mfma(h2, bd[2], z));
      ST_SC1((v4u{nosent(d[0]), nosent(d[1]), nosent(d[2]), nosent(d[3])}), o_dst);
    }
  }
  STAMP(15);
  if (J < J_F_END) {
    const int f = 4 * X + (J - J_C);
    float gb = bf_lo(*(LAS unsigned short*)(L + L_GATE + g * 4096 + 3072 + (16 * f + n) * 2));
    float x2 = bf_lo(*(LAS unsigned short*)(L + L_X + (g * 512 + 16 * f + n) * 2));
    int o_src = WS_DOWN + (4 * w + g) * 8192 + (16 * f + n) * 16, o_wr = L_RED + w * 256 + lane * 16, o_rd = L_RED + n * 16 + g * 4, o_out = (g * 512 + 16 * f + n) * 2;
    PIN(gb); PIN(x2); PIN(o_src); PIN(o_wr); PIN(o_rd); PIN(o_out);
    if (w < 7) {
      f32x4 s;
      v4u dv[2];
      GATHER(2, dv, o_src + i * 28 * 8192);
#pragma unroll
      for (int b = 0; b < 4; ++b) s[b] = x32_sum(x16_sum(__uint_as_float(dv[0][b]) + __uint_as_float(dv[1][b])));
      if (lane < 16) *(LAS f32x4*)(L + o_wr) = s;
    }
    bar();
    if (w == 0) {
      float q7[7];
#pragma unroll
      for (int ww = 0; ww < 7; ++ww) q7[ww] = *(LAS float*)(L + o_rd + ww * 256);
      const float y = rb(((q7[0] + q7[1]) + (q7[2] + q7[3])) + ((q7[4] + q7[5]) + q7[6]));
      const unsigned ob = pk2(x2 + rb(gb * y), 0.f) & 0xffffu;
      const unsigned o2 = ob | (dppu<XOR1>(ob) << 16);
      if ((n & 1) == 0) *(GAS unsigned*)((GAS unsigned char*)p.out + o_out) = o2;
    }
  }
  STAMP(16); DUMP();
#undef tid
#undef lane
#undef g
#undef n
}
}

extern "C" void kernel_launch(void* const* d_in, const int* in_sizes, int n_in, void* d_out, int out_size, void* d_ws, size_t ws_size, hipStream_t stream) {
  (void)in_sizes; (void)n_in; (void)out_size; (void)ws_size;
  Params p;
  p.x = d_in[0]; p.k = d_in[1]; p.v = d_in[2]; p.wq = d_in[3]; p.wo = d_in[4];
  p.wg = d_in[6]; p.wu = d_in[7]; p.wd = d_in[8]; p.c = d_in[9]; p.wm = d_in[10];
  p.out = d_out; p.ws = d_ws;
  hipLaunchKernelGGL(adaln_block_kernel, dim3(NCU), dim3(NTHR), 0, stream, p);
}
```

```cpp
#include <hip/hip_runtime.h>
#include <hip/hip_bf16.h>
#include <stdint.h>

#define GAS __attribute__((address_space(1)))
#define LAS __attribute__((address_space(3)))
typedef unsigned v4u __attribute__((ext_vector_type(4)));
typedef unsigned v2u __attribute__((ext_vector_type(2)));
typedef float f32x4 __attribute__((ext_vector_type(4)));
typedef short s16x4 __attribute__((ext_vector_type(4)));
typedef __bf16 bf16x8 __attribute__((ext_vector_type(8)));
typedef __bf16 bf16x2 __attribute__((ext_vector_type(2)));
typedef LAS unsigned char* lptr;

#define PACE 5
#define FIRST 1
#define PACE_H PACE
#define LATE_WD 0
#define PA PACE
#define PB PACE
#define PRETOUCH 1
#define W_GATE 1
#define W_AT 3
#define EARLY_V2 0
#define STAMPS 0

namespace {
constexpr int NCU = 256, NTHR = 512;
constexpr unsigned KV_BYTES = 4u * 8u * 8192u * 128u * 2u;
constexpr unsigned WS_EPOCH = 0, WS_READY = 4096, WS_H = 8192, WS_MP = 16384,
                   WS_QP = WS_MP + 48 * 4 * 2048,
                   WS_GATE = WS_QP + 8 * 4 * 4 * 2048,
                   WS_STAT = WS_GATE + 16384, WS_ATT = WS_STAT + 8192,
                   WS_PART = WS_ATT + 8 * 32768,
                   WS_DOWN = WS_PART + 256 * 1024, WS_END = WS_DOWN + 56 * 8192, WS_STAMP = WS_END + 65536;
constexpr int RS = 1088;
constexpr int L_WM = 0, L_WQ = 16384, L_WD = 0, L_W = 32768, L_MRG = 98304, L_GATE = L_MRG + 18432, L_ACT = L_GATE + 16384, L_ACTH = L_ACT + 4352, L_X = L_ACTH + 4352,
              L_Q = L_X + 4096, L_RED = L_Q + 1024, L_MISC = L_RED + 8192, L_BYTES = L_MISC + 1024;
constexpr int MRG_H = 576, MRG_W = 4 * MRG_H;
constexpr int M_LN = L_MISC, M_MX = L_MISC + 128, M_LS = L_MISC + 256, M_HID = L_MISC + 384, M_FLAG = L_MISC + 640, M_CLR = L_MISC + 644, M_CNT = L_MISC + 648, M_CN2 = L_MISC + 652, M_FW = L_MISC + 768, M_ZERO = L_MISC + 896;
constexpr unsigned SENT = 0xFFFFFFFFu;
constexpr unsigned SPIN_MAX = 1u << 15;
constexpr int AUX_NT = 2, AUX_SC1 = 16;
constexpr float LOG2E = 1.4426950408889634f;
constexpr int J_G = 8, J_H = 24, J_GR_END = 28;
constexpr int J_B = 8, J_C = 16, J_F_END = 20, J_C_END = 23;

struct Params { const void* x; const void* k; const void* v; const void* wq; const void* wo; const void* wg; const void* wu; const void* wd; const void* c; const void* wm; void* out; void* ws; };

__device__ __forceinline__ float bf_lo(unsigned u) { return __builtin_bit_cast(float, u << 16); }
__device__ __forceinline__ float bf_hi(unsigned u) { return __builtin_bit_cast(float, u & 0xffff0000u); }
__device__ __forceinline__ unsigned pk2(float a, float b) { bf16x2 r = {(__bf16)a, (__bf16)b}; return __builtin_bit_cast(unsigned, r); }
__device__ __forceinline__ float rb(float a) { return bf_lo(pk2(a, a)); }
template <int CTRL> __device__ __forceinline__ float dpp(float x) { return __builtin_bit_cast(float, __builtin_amdgcn_mov_dpp(__builtin_bit_cast(int, x), CTRL, 0xf, 0xf, true)); }
template <int CTRL> __device__ __forceinline__ unsigned dppu(unsigned x) { return (unsigned)__builtin_amdgcn_mov_dpp((int)x, CTRL, 0xf, 0xf, true); }
constexpr int XOR1 = 0xB1, XOR2 = 0x4E, HMIR = 0x141, ROR4 = 0x124, ROR8 = 0x128;
__device__ __forceinline__ float x16_sum(float x) { auto s = __builtin_amdgcn_permlane16_swap(__float_as_uint(x), __float_as_uint(x), false, false); return __uint_as_float(s[0]) + __uint_as_float(s[1]); }
__device__ __forceinline__ float x32_sum(float x) { auto s = __builtin_amdgcn_permlane32_swap(__float_as_uint(x), __float_as_uint(x), false, false); return __uint_as_float(s[0]) + __uint_as_float(s[1]); }
__device__ __forceinline__ float x16_max(float x) { auto s = __builtin_amdgcn_permlane16_swap(__float_as_uint(x), __float_as_uint(x), false, false); return fmaxf(__uint_as_float(s[0]), __uint_as_float(s[1])); }
__device__ __forceinline__ float x32_max(float x) { auto s = __builtin_amdgcn_permlane32_swap(__float_as_uint(x), __float_as_uint(x), false, false); return fmaxf(__uint_as_float(s[0]), __uint_as_float(s[1])); }
__device__ __forceinline__ float row_sum(float x) { x += dpp<XOR1>(x); x += dpp<XOR2>(x); x += dpp<HMIR>(x); x += dpp<ROR8>(x); return x; }
__device__ __forceinline__ float wave_sum(float x) { return x32_sum(x16_sum(row_sum(x))); }
__device__ __forceinline__ void wave_sum2(float& x, float& y) {
  x += dpp<XOR1>(x); y += dpp<XOR1>(y); x += dpp<XOR2>(x); y += dpp<XOR2>(y); x += dpp<HMIR>(x); y += dpp<HMIR>(y); x += dpp<ROR8>(x); y += dpp<ROR8>(y);
  x = x16_sum(x); y = x16_sum(y); x = x32_sum(x); y = x32_sum(y);
}
__device__ __forceinline__ f32x4 mfma(v4u a, v4u b, f32x4 c) { return __builtin_amdgcn_mfma_f32_16x16x32_bf16(__builtin_bit_cast(bf16x8, a), __builtin_bit_cast(bf16x8, b), c, 0, 0, 0); }
__device__ __forceinline__ v2u tr_read(lptr p) { s16x4 r = __builtin_amdgcn_ds_read_tr16_b64_v4i16((LAS s16x4*)p); return __builtin_bit_cast(v2u, r); }
__device__ __forceinline__ v4u tr_frag(lptr p0, lptr p1) { v2u a = tr_read(p0), b = tr_read(p1); return v4u{a.x, a.y, b.x, b.y}; }
__device__ __forceinline__ v4u lds128(lptr p) { return *(LAS v4u*)p; }
__device__ __forceinline__ void bar() { asm volatile("s_waitcnt lgkmcnt(0)\n\ts_barrier" ::: "memory"); }
__device__ __forceinline__ void glds16(const void* gsrc, unsigned lds_dst) {
  unsigned keep;
  lds_dst = __builtin_amdgcn_readfirstlane(lds_dst);
  asm volatile("s_mov_b32 %0, m0\n\ts_mov_b32 m0, %2\n\ts_nop 0\n\tglobal_load_lds_dwordx4 %1, off nt\n\ts_mov_b32 m0, %0" : "=&s"(keep) : "v"(gsrc), "s"(lds_dst) : "memory");
}
__device__ __forceinline__ unsigned nosent(float f) { return min(__float_as_uint(f), SENT - 1u); }
__device__ __forceinline__ unsigned nosentu(unsigned u) { return min(u, SENT - 1u); }
__device__ __forceinline__ unsigned umax4(v4u a) { return max(max(a.x, a.y), max(a.z, a.w)); }
__device__ __forceinline__ bool all_lanes(bool ok) { return __builtin_amdgcn_ballot_w64(!ok) == 0ull; }
__device__ __forceinline__ float sel4(f32x4 d, int g) { const bool o = (g & 1) != 0; const float a = o ? d[1] : d[0], b = o ? d[3] : d[2]; return (g & 2) ? b : a; }
__device__ __forceinline__ bool isnan_(float x) { return x != x; }
#define PIN(x) asm volatile("" : "+v"(x))
__device__ __forceinline__ float fast_rcp(float x) { return __builtin_amdgcn_rcpf(x); }
#define RSRC(p, n) __builtin_amdgcn_make_buffer_rsrc((void*)(p), 0, (int)(n), 0x00020000)
#define LD_SC1(off) __builtin_amdgcn_raw_buffer_load_b128(rws, (off), 0, AUX_SC1)
#define ST_SC1(v, off) __builtin_amdgcn_raw_buffer_store_b128((v), rws, (off), 0, AUX_SC1)
#define FENCE() __builtin_amdgcn_sched_barrier(0)
#define LD8_SC1(off) __builtin_amdgcn_raw_buffer_load_b64(rws, (off), 0, AUX_SC1)
#define GATHER8(N, V, OFF) do { \
    unsigned pend_ = 0; \
    _Pragma("unroll") for (int i = 0; i < (N); ++i) V[i] = LD8_SC1(OFF); \
    _Pragma("unroll") for (int i = 0; i < (N); ++i) pend_ |= all_lanes(max(V[i].x, V[i].y) != SENT) ? 0u : 1u << i; \
    for (unsigned spin_ = 0; pend_ != 0u && spin_ < SPIN_MAX; ++spin_) { \
      asm volatile("" ::: "memory"); \
      _Pragma("unroll") for (int i = 0; i < (N); ++i) if (pend_ & (1u << i)) V[i] = LD8_SC1(OFF); \
      _Pragma("unroll") for (int i = 0; i < (N); ++i) if (pend_ & (1u << i)) { if (all_lanes(max(V[i].x, V[i].y) != SENT)) pend_ &= ~(1u << i); } \
    } } while (0)
#define GATHER(N, V, OFF) do { \
    unsigned pend_ = 0; \
    _Pragma("unroll") for (int i = 0; i < (N); ++i) V[i] = LD_SC1(OFF); \
    _Pragma("unroll") for (int i = 0; i < (N); ++i) pend_ |= all_lanes(umax4(V[i]) != SENT) ? 0u : 1u << i; \
    for (unsigned spin_ = 0; pend_ != 0u && spin_ < SPIN_MAX; ++spin_) { \
      asm volatile("" ::: "memory"); \
      _Pragma("unroll") for (int i = 0; i < (N); ++i) if (pend_ & (1u << i)) V[i] = LD_SC1(OFF); \
      _Pragma("unroll") for (int i = 0; i < (N); ++i) if (pend_ & (1u << i)) { if (all_lanes(umax4(V[i]) != SENT)) pend_ &= ~(1u << i); } \
    } } while (0)
#define STAMP(i) do { } while (0)
#define DUMP() do { } while (0)

__device__ __forceinline__ int img_chunk(int lane, int row) { return ((((lane & 7) >> 1) ^ ((row >> 1) & 3)) << 1) | (lane & 1); }
__device__ __forceinline__ int frag_base(int lane, int ct) { const int g = lane >> 4, q = (lane >> 2) & 3; return (4 * g + q) * 128 + ((ct ^ ((2 * g + (q >> 1)) & 3)) * 32) + (lane & 3) * 8; }
__device__ __forceinline__ v4u w_frag(lptr base, int s) { return tr_frag(base + s * 4096, base + s * 4096 + 2048); }
__device__ __forceinline__ int act_off(int k) { return (k >> 5) * 64 + ((k >> 2) & 3) * 16 + ((k >> 4) & 1) * 8; }
__device__ __forceinline__ void ln_consts(float sum, float sumsq, float& mean_r, float& rden) {
  const float mean = sum * (1.f / 512.f);
  const float var = fmaxf(sumsq * (1.f / 512.f) - mean * mean, 0.f);
  mean_r = rb(mean);
  rden = fast_rcp(rb(__builtin_amdgcn_sqrtf(rb(rb(var) + 1e-5f))));
}
__device__ __forceinline__ float ln_mod(float x, float mean_r, float rden, float c1, float ss) {
  return rb(rb(rb(rb(x - mean_r) * rden) * c1) + ss);
}

__global__ void __launch_bounds__(NTHR, 2) adaln_block_kernel(Params p) {
  __shared__ __attribute__((aligned(16))) unsigned char smem[L_BYTES + (STAMPS ? 1024 : 0)];
  lptr L = (lptr)smem;
  const unsigned lds0 = (unsigned)(uintptr_t)L;
  const int tid = threadIdx.x, lane = tid & 63, w = __builtin_amdgcn_readfirstlane(tid >> 6);
  const int cu = blockIdx.x, X = cu & 7, J = cu >> 3;
  const int g = lane >> 4, n = lane & 15;
  const auto rws = RSRC(p.ws, WS_END);
  const auto rk = RSRC(p.k, KV_BYTES);
  const auto rv = RSRC(p.v, KV_BYTES);
  const bool roleM = J < J_H, roleH = J >= J_H, roleGR = J >= J_H && J < J_GR_END, roleB = J >= J_B && J < J_C, roleC = J >= J_C && J < J_C_END;
  const int kq = J & 3;
  STAMP(0);
  const int mg = J < J_G ? 2 * X + (J >> 2) : 16 + 4 * X + ((J - J_G) >> 2);
  {
    const int r0 = 16 * w + (lane >> 3), ch = img_chunk(lane, r0);
    if (roleM) {
      const GAS unsigned char* s = (const GAS unsigned char*)p.wm + ((size_t)(128 * kq + r0) * 3072 + 64 * mg + 8 * ch) * 2;
      glds16((const void*)s, lds0 + L_WM + w * 2048);
      glds16((const void*)(s + 8 * 3072 * 2), lds0 + L_WM + w * 2048 + 1024);
    }
    const GAS unsigned char* s = (const GAS unsigned char*)p.wq + ((size_t)(128 * kq + r0) * 4096 + 64 * (8 * X + (J >> 2)) + 8 * ch) * 2;
    glds16((const void*)s, lds0 + L_WQ + w * 2048);
    glds16((const void*)(s + 8 * 4096 * 2), lds0 + L_WQ + w * 2048 + 1024);
  }
  const unsigned long long ep = __hip_atomic_load((GAS unsigned long long*)p.ws, __ATOMIC_RELAXED, __HIP_MEMORY_SCOPE_AGENT);
  v2u cv = {0u, 0u};
  if (roleM && tid < 128) cv = *(const GAS v2u*)((const GAS unsigned char*)p.c + ((tid >> 5) * 512 + 128 * kq + 4 * (tid & 31)) * 2);
  asm volatile("s_barrier" ::: "memory");
  if (w == 1) {
    const v4u sent = {SENT, SENT, SENT, SENT};
    ST_SC1(sent, WS_QP + ((X * 4 + g) * 4 + kq) * 2048 + (J >> 2) * 256 + n * 16);
    ST_SC1(sent, WS_PART + cu * 1024 + lane * 16);
    if (lane < 2) ST_SC1(sent, WS_STAT + cu * 32 + lane * 16);
    if (roleB) {
#pragma unroll
      for (int e = 0; e < 4; ++e) ST_SC1(sent, WS_ATT + (2 * e + (lane >> 5)) * 32768 + ((X * 4 + ((lane >> 3) & 3)) * 512 + 64 * (J - J_B)) * 2 + (lane & 7) * 16);
    }
    if (roleGR && n < 8) ST_SC1(sent, WS_GATE + g * 4096 + (4 * X + (J - J_H)) * 128 + n * 16);
    if (roleC) {
#pragma unroll
      for (int e = 0; e < 8; ++e) ST_SC1(sent, WS_DOWN + (7 * X + (J - J_C)) * 8192 + e * 1024 + lane * 16);
    }
  }
  FENCE();
  const bool roleT = roleB || roleC;
  const unsigned kvbase = (((unsigned)(J >> 3) * 8u + (unsigned)X) * 8192u + (unsigned)(J & 7) * 1024u) * 256u + (unsigned)w * 4096u;
  const unsigned koff = (4 * (lane & 3) + ((lane >> 2) & 1)) * 256 + (lane >> 3) * 16;
  v4u kf[8][4];
#define K_ISSUE(t) do { unsigned ko_ = koff; asm volatile("" : "+v"(ko_)); _Pragma("unroll") for (int q_ = 0; q_ < 4; ++q_) kf[t][q_] = __builtin_amdgcn_raw_buffer_load_b128(rk, ko_ + (q_ >> 1) * 512 + (q_ & 1) * 128, kvbase + (t) * 32768, AUX_NT); FENCE(); } while (0)
  K_ISSUE(0);
  if (FIRST > 1) K_ISSUE(1);
  if (w == 2 && PRETOUCH) {
    const GAS unsigned char* a;
    if (roleB) a = (const GAS unsigned char*)p.wo + (size_t)(512 * X + 8 * lane) * 1024;
    else if (roleC) a = (const GAS unsigned char*)(lane < 32 ? p.wg : p.wu) + (size_t)(lane & 31) * (16 * 3584);
    else a = (const GAS unsigned char*)p.v + kvbase + (lane & 7) * 32768;
    glds16((const void*)a, lds0 + L_RED + 4096);
    if (roleC) {
      const GAS unsigned char* c2 = lane < 32 ? (const GAS unsigned char*)p.wd + (size_t)(32 * (7 * X + (J - J_C)) + lane) * 1024 : lane < 48 ? (const GAS unsigned char*)p.out + (lane & 3) * 1024 : (const GAS unsigned char*)p.x + (lane & 3) * 1024;
      glds16((const void*)c2, lds0 + L_RED + 5120);
    }
  }
  if (tid == 0) { *(LAS unsigned*)(L + M_FLAG) = 0u; *(LAS unsigned*)(L + M_CLR) = 0u; *(LAS unsigned*)(L + M_CNT) = 0u; *(LAS unsigned*)(L + M_CN2) = 0u; }
  const unsigned long long E = ep + 1ull;
  const unsigned ta = __builtin_amdgcn_readfirstlane((unsigned)E), tb2 = __builtin_amdgcn_readfirstlane(((unsigned)E * 0x9E3779B1u) ^ (unsigned)(E >> 32));
  if (roleM && tid < 128) {
    float cs[4] = {bf_lo(cv.x), bf_hi(cv.x), bf_lo(cv.y), bf_hi(cv.y)};
#pragma unroll
    for (int e = 0; e < 4; ++e) cs[e] = cs[e] / (1.f + expf(-cs[e]));
    *(LAS v2u*)(L + L_ACT + (tid >> 5) * RS + act_off(4 * (tid & 31))) = v2u{pk2(cs[0], cs[1]), pk2(cs[2], cs[3])};
  }
  STAMP(17);
  asm volatile("s_waitcnt vmcnt(%0)" ::"i"(4 * FIRST) : "memory");
  STAMP(18);
  if (w == 1) {
    if (lane == 0) ST_SC1((v4u{0u, ta, 0u, tb2}), WS_READY + cu * 16);
  }
  bar();
  if (w == 1 && lane == 0) *(volatile LAS unsigned*)(L + M_CLR) = 1u;
  STAMP(1);
  if (roleM && w < 4) {
    lptr ap = L + L_ACT + (lane & 3) * RS + g * 16;
    lptr wp = L + L_WM + frag_base(lane, w);
    f32x4 a0 = mfma(lds128(ap), w_frag(wp, 0), f32x4{0.f, 0.f, 0.f, 0.f}), a1 = mfma(lds128(ap + 64), w_frag(wp, 1), f32x4{0.f, 0.f, 0.f, 0.f});
    a0 = mfma(lds128(ap + 128), w_frag(wp, 2), a0); a1 = mfma(lds128(ap + 192), w_frag(wp, 3), a1);
    const float v = sel4(a0 + a1, g);
    const float vn = dpp<XOR1>(v);
    if ((n & 1) == 0) ST_SC1((v4u{__float_as_uint(v), ta, __float_as_uint(vn), tb2}), WS_MP + ((mg * 4 + kq) * 4 + g) * 512 + (8 * w + (n >> 1)) * 16);
    FENCE();
  }
  if (roleH && w == 0) {
    __builtin_amdgcn_s_setprio(2);
    const int eta = 8 * X + (J - J_H), b = (lane >> 2) & 3, pr = lane & 3;
    v4u xq[4];
#pragma unroll
    for (int i = 0; i < 4; ++i) xq[i] = *(const GAS v4u*)((const GAS unsigned char*)p.x + (g * 512 + 32 * n + 8 * i) * 2);
    const unsigned xp = *(const GAS unsigned*)((const GAS unsigned char*)p.x + (b * 512 + 8 * eta + 2 * pr) * 2);
    float s1 = 0.f, s2 = 0.f;
#pragma unroll
    for (int i = 0; i < 4; ++i)
#pragma unroll
      for (int e = 0; e < 4; ++e) { const float a = bf_lo(xq[i][e]), c = bf_hi(xq[i][e]); s1 += a + c; s2 += a * a + c * c; }
    s1 = row_sum(s1); s2 = row_sum(s2);
    float mean_r, rden;
    ln_consts(s1, s2, mean_r, rden);
    mean_r = __builtin_bit_cast(float, __builtin_amdgcn_ds_bpermute(64 * b, __builtin_bit_cast(int, mean_r)));
    rden = __builtin_bit_cast(float, __builtin_amdgcn_ds_bpermute(64 * b, __builtin_bit_cast(int, rden)));
    v4u ms, mc;
    for (unsigned spin = 0; spin < SPIN_MAX; ++spin) {
      ms = LD_SC1(WS_MP + (((eta >> 3) * 4 + g) * 4 + b) * 512 + ((eta & 7) * 4 + pr) * 16);
      mc = LD_SC1(WS_MP + (((8 + (eta >> 3)) * 4 + g) * 4 + b) * 512 + ((eta & 7) * 4 + pr) * 16);
      if (all_lanes((ms.y == ta) & (ms.w == tb2) & (mc.y == ta) & (mc.w == tb2))) break;
      asm volatile("" ::: "memory");
    }
    const float sa0 = rb(x32_sum(x16_sum(__uint_as_float(ms.x)))), sa1 = rb(x32_sum(x16_sum(__uint_as_float(ms.z))));
    const float ca0 = rb(x32_sum(x16_sum(__uint_as_float(mc.x)))), ca1 = rb(x32_sum(x16_sum(__uint_as_float(mc.z))));
    const unsigned hp = pk2(ln_mod(bf_lo(xp), mean_r, rden, rb(1.f + ca0), sa0), ln_mod(bf_hi(xp), mean_r, rden, rb(1.f + ca1), sa1));
    const unsigned hn = dppu<XOR1>(hp);
    if (lane < 16 && (pr & 1) == 0) ST_SC1((v4u{hp, ta, hn, tb2}), WS_H + eta * 128 + (b * 2 + (pr >> 1)) * 16);
    __builtin_amdgcn_s_setprio(0);
    FENCE();
  }
  STAMP(2);
#define PACE_WAITN(x) do { if (w != 0 && PACE >= 0) asm volatile("s_waitcnt vmcnt(%0)" ::"i"(PACE < 0 ? 0 : PACE + (x)) : "memory"); } while (0)
#define PACE_A() do { if (w != 0) asm volatile("s_waitcnt vmcnt(%0)" ::"i"(PA) : "memory"); } while (0)
#define PACE_B() do { if (w != 0) asm volatile("s_waitcnt vmcnt(%0)" ::"i"(PB) : "memory"); } while (0)
#define PACE_WAIT2() PACE_WAITN(4)
#define PACE_WAIT() PACE_WAITN(0)
  if (FIRST < 2) K_ISSUE(1);

  PACE_A(); K_ISSUE(2); PACE_A(); K_ISSUE(3);
  if (w == 0) {
    __builtin_amdgcn_s_setprio(2);
    v4u bq[4][4];
#pragma unroll
    for (int ct = 0; ct < 4; ++ct) { lptr wp = L + L_WQ + frag_base(lane, ct);
#pragma unroll
      for (int s = 0; s < 4; ++s) bq[ct][s] = w_frag(wp, s); }
    v4u hv[2];
    for (unsigned spin = 0; spin < SPIN_MAX; ++spin) {
      bool ok = true;
#pragma unroll
      for (int i = 0; i < 2; ++i) { hv[i] = LD_SC1(WS_H + (128 * kq + 64 * i + lane) * 16); ok &= (hv[i].y == ta) & (hv[i].w == tb2); }
#pragma unroll
      for (int i = 0; i < 4; ++i) { const v4u rd = LD_SC1(WS_READY + (64 * i + lane) * 16); ok &= (rd.y == ta) & (rd.w == tb2); }
      if (all_lanes(ok)) break;
      asm volatile("" ::: "memory");
    }
    STAMP(3);
#pragma unroll
    for (int i = 0; i < 2; ++i) { const int gm = 64 * i + lane; *(LAS v2u*)(L + L_ACTH + ((gm >> 1) & 3) * RS + act_off(8 * (gm >> 3) + 4 * (gm & 1))) = v2u{hv[i].x, hv[i].z}; }
    asm volatile("s_waitcnt lgkmcnt(0)" ::: "memory");
    lptr ap = L + L_ACTH + (lane & 3) * RS + g * 16;
    const v4u a0 = lds128(ap), a1 = lds128(ap + 64), a2 = lds128(ap + 128), a3 = lds128(ap + 192);
    float qv[4];
#pragma unroll
    for (int ct = 0; ct < 4; ++ct) {
      const f32x4 z = {0.f, 0.f, 0.f, 0.f};
      qv[ct] = sel4(mfma(a1, bq[ct][1], mfma(a0, bq[ct][0], z)) + mfma(a3, bq[ct][3], mfma(a2, bq[ct][2], z)), g);
    }
    while (*(volatile LAS unsigned*)(L + M_CLR) == 0u) __builtin_amdgcn_s_sleep(1);
    ST_SC1((v4u{nosent(qv[0]), nosent(qv[1]), nosent(qv[2]), nosent(qv[3])}), WS_QP + ((X * 4 + g) * 4 + kq) * 2048 + (J >> 2) * 256 + n * 16);
    if (cu == 0 && lane == 0) __hip_atomic_store((GAS unsigned long long*)p.ws, E, __ATOMIC_RELAXED, __HIP_MEMORY_SCOPE_AGENT);
    __builtin_amdgcn_s_setprio(0);
    FENCE();
    STAMP(4);
  }
  PACE_B(); K_ISSUE(4); PACE_B(); K_ISSUE(5);
  PACE_WAIT(); K_ISSUE(6); PACE_WAIT(); K_ISSUE(7);
  if (w == 0) {
    __builtin_amdgcn_s_setprio(2);
    v4u qp[8];
    for (unsigned spin = 0; spin < SPIN_MAX; ++spin) {
      unsigned m = 0;
#pragma unroll
      for (int i = 0; i < 8; ++i) { qp[i] = LD_SC1(WS_QP + ((X * 4 + (J >> 3)) * 4 + (i >> 1)) * 2048 + ((i & 1) * 64 + lane) * 16); m = max(m, umax4(qp[i])); }
      if (all_lanes(m != SENT)) break;
      asm volatile("" ::: "memory");
    }
#pragma unroll
    for (int hf = 0; hf < 2; ++hf)
#pragma unroll
      for (int ct = 0; ct < 4; ++ct) {
        const float s = (__uint_as_float(qp[hf][ct]) + __uint_as_float(qp[2 + hf][ct])) + (__uint_as_float(qp[4 + hf][ct]) + __uint_as_float(qp[6 + hf][ct]));
        *(LAS unsigned short*)(L + L_Q + (64 * (4 * hf + g) + 16 * ct + n) * 2) = (unsigned short)(pk2(s, s) & 0xffffu);
      }
    asm volatile("s_waitcnt lgkmcnt(0)" ::: "memory");
    STAMP(5);
    if (roleGR) {
      const int gg = 4 * X + (J - J_H);
      v4u gv[8];
      for (unsigned spin = 0; spin < SPIN_MAX; ++spin) {
        bool ok = true;
#pragma unroll
        for (int i = 0; i < 8; ++i) { gv[i] = LD_SC1(WS_MP + (((16 + gg) * 4 + (i >> 1)) * 4 + g) * 512 + ((i & 1) * 16 + n) * 16); ok &= (gv[i].y == ta) & (gv[i].w == tb2); }
        if (all_lanes(ok)) break;
        asm volatile("" ::: "memory");
      }
#pragma unroll
      for (int hf = 0; hf < 2; ++hf) {
        const float s0 = (__uint_as_float(gv[hf].x) + __uint_as_float(gv[2 + hf].x)) + (__uint_as_float(gv[4 + hf].x) + __uint_as_float(gv[6 + hf].x));
        const float s1 = (__uint_as_float(gv[hf].z) + __uint_as_float(gv[2 + hf].z)) + (__uint_as_float(gv[4 + hf].z) + __uint_as_float(gv[6 + hf].z));
        __builtin_amdgcn_raw_buffer_store_b32(nosentu(pk2(s0, s1)), rws, WS_GATE + g * 4096 + gg * 128 + (16 * hf + n) * 4, 0, AUX_SC1);
      }
    }
    __builtin_amdgcn_s_setprio(0);
    FENCE();
  }
  v4u vf[4][8];
#define V_ISSUE(u) do { unsigned vo_ = lane * 16; asm volatile("" : "+v"(vo_)); _Pragma("unroll") for (int j_ = 0; j_ < 8; ++j_) vf[u][j_] = __builtin_amdgcn_raw_buffer_load_b128(rv, vo_ + (j_ & 3) * 1024, kvbase + (2 * (u) + (j_ >> 2)) * 32768, AUX_NT); FENCE(); } while (0)
  PACE_WAIT(); V_ISSUE(0);
  PACE_WAIT2(); V_ISSUE(1);
  STAMP(6);
  bar();
  STAMP(7);
  const v4u qf0 = lds128(L + L_Q + (n & 3) * 256 + g * 32 + ((n >> 2) & 1) * 16), qf1 = lds128(L + L_Q + (n & 3) * 256 + 128 + g * 32 + ((n >> 2) & 1) * 16);
  float S[8][4];
#define QK(t) do { const f32x4 z_ = {0.f, 0.f, 0.f, 0.f}; \
    const f32x4 d0_ = mfma(kf[t][1], qf1, mfma(kf[t][0], qf0, z_)), d1_ = mfma(kf[t][3], qf1, mfma(kf[t][2], qf0, z_)); \
    _Pragma("unroll") for (int r_ = 0; r_ < 4; ++r_) { \
      auto s_ = __builtin_amdgcn_permlane32_swap(__float_as_uint(d0_[r_]), __float_as_uint(d1_[r_]), false, false); \
      S[t][r_] = __uint_as_float(s_[0]) + dpp<ROR4>(__uint_as_float(s_[1])); } } while (0)
  QK(0); QK(1); FENCE(); V_ISSUE(2);
  QK(2); QK(3); FENCE(); V_ISSUE(3);
  if (W_GATE && lane == 0) (void)__hip_atomic_fetch_add((LAS unsigned*)(L + M_CN2), 1u, __ATOMIC_RELAXED, __HIP_MEMORY_SCOPE_WORKGROUP);
  QK(4); QK(5); QK(6); QK(7); FENCE();
  STAMP(8);
  unsigned S2[8][2];
  float mx = -__builtin_inff();
#pragma unroll
  for (int t = 0; t < 8; ++t)
#pragma unroll
    for (int r = 0; r < 2; ++r) {
      const unsigned a = pk2(S[t][2 * r], S[t][2 * r + 1]);
      S2[t][r] = pk2(bf_lo(a) * 0.08838834764831845f, bf_hi(a) * 0.08838834764831845f);
      mx = fmaxf(mx, fmaxf(bf_lo(S2[t][r]), bf_hi(S2[t][r])));
    }
  FENCE();
  FENCE();
  mx = x32_max(x16_max(mx));
  float ls = 0.f;
  v4u pf[4];
#pragma unroll
  for (int u = 0; u < 4; ++u) {
    float e[8];
#pragma unroll
    for (int j = 0; j < 8; ++j) { const unsigned sp = S2[2 * u + (j >> 2)][(j & 3) >> 1]; e[j] = __builtin_amdgcn_exp2f((((j & 1) ? bf_hi(sp) : bf_lo(sp)) - mx) * LOG2E); ls += e[j]; }
    pf[u] = v4u{pk2(e[0], e[1]), pk2(e[2], e[3]), pk2(e[4], e[5]), pk2(e[6], e[7])};
  }
  ls = x32_sum(x16_sum(ls));
  if (lane < 4) { *(LAS float*)(L + M_MX + lane * 32 + w * 4) = mx; *(LAS float*)(L + M_LS + lane * 32 + w * 4) = ls; }
  {
    asm volatile("s_waitcnt lgkmcnt(0)" ::: "memory");
    unsigned old = 0;
    if (lane == 0) old = __hip_atomic_fetch_add((LAS unsigned*)(L + M_CNT), 1u, __ATOMIC_RELAXED, __HIP_MEMORY_SCOPE_WORKGROUP);
    if (__builtin_amdgcn_readfirstlane(old) == 7u) {
      const float mw = *(volatile LAS float*)(L + M_MX + ((lane >> 3) & 3) * 32 + (lane & 7) * 4), lw = *(volatile LAS float*)(L + M_LS + ((lane >> 3) & 3) * 32 + (lane & 7) * 4);
      float M = fmaxf(mw, dpp<XOR1>(mw)); M = fmaxf(M, dpp<XOR2>(M)); M = fmaxf(M, dpp<HMIR>(M));
      const float f = __builtin_amdgcn_exp2f((mw - M) * LOG2E);
      float Lc = f * lw; Lc += dpp<XOR1>(Lc); Lc += dpp<XOR2>(Lc); Lc += dpp<HMIR>(Lc);
      if (lane < 32) *(LAS float*)(L + M_FW + lane * 4) = f;
      if (lane < 32 && (lane & 7) == 0) __builtin_amdgcn_raw_buffer_store_b64((v2u{nosent(M), nosent(Lc)}), rws, WS_STAT + cu * 32 + (lane >> 3) * 8, 0, AUX_SC1);
    }
  }
  FENCE();
  STAMP(9);
  v4u wr[8];
#define W_LOADS() do { \
     \
    const int pc_ = 7 * X + (J - J_C); \
    const auto rwa = RSRC(roleB ? p.wo : p.wg, roleB ? 4096 * 512 * 2 : roleC ? 512 * 1792 * 2 : 0); \
    const auto rwb = RSRC(roleB ? p.wo : p.wu, roleB ? 4096 * 512 * 2 : roleC ? 512 * 1792 * 2 : 0); \
    const int rB = 64 * w + (lane >> 3); \
    const int rS = 128 * (w >> 1) + 32 * ((lane >> 5) & 1) + 8 * ((lane >> 3) & 3) + 4 * (w & 1); \
    const unsigned vo = roleB ? ((512 * X + rS) * 512 + 64 * (J - J_B) + 8 * img_chunk(lane, rB)) * 2 : ((64 * w + (lane >> 2)) * 1792 + 32 * pc_ + 8 * (lane & 3)) * 2; \
_Pragma("unroll") \
    for (int e = 0; e < 4; ++e) wr[e] = __builtin_amdgcn_raw_buffer_load_b128(rwa, vo, roleB ? (64 * (e & 1) + (e >> 1)) * 1024 : e * 16 * 1792 * 2, AUX_NT); \
_Pragma("unroll") \
    for (int e = 0; e < 4; ++e) wr[4 + e] = __builtin_amdgcn_raw_buffer_load_b128(rwb, vo, roleB ? (64 * (e & 1) + 2 + (e >> 1)) * 1024 : e * 16 * 1792 * 2, AUX_NT); \
  } while (0)
  f32x4 oa[8];
#pragma unroll
  for (int d = 0; d < 8; ++d) oa[d] = f32x4{0.f, 0.f, 0.f, 0.f};
#define PV(u) do { _Pragma("unroll") for (int d = 0; d < 8; ++d) { \
      const unsigned sel = (d & 1) ? 0x07060302u : 0x05040100u; v4u a; \
      _Pragma("unroll") for (int i = 0; i < 4; ++i) a[i] = __builtin_amdgcn_perm(vf[u][2 * i + 1][d >> 1], vf[u][2 * i][d >> 1], sel); \
      oa[d] = mfma(a, pf[u], oa[d]); } FENCE(); } while (0)
  PV(0); PV(1);
  if (W_AT == 1) W_LOADS();
  PV(2);
  if (W_AT == 2) W_LOADS();
  PV(3);
  if (W_AT == 3) {
    if (W_GATE && roleT) { for (unsigned spin = 0; spin < (SPIN_MAX << 4); ++spin) { if (*(volatile LAS unsigned*)(L + M_CN2) >= 8u) break; __builtin_amdgcn_s_sleep(1); } }
    W_LOADS(); FENCE();
  }
  STAMP(10);
  int tid2 = threadIdx.x; asm volatile("" : "+v"(tid2));
#define tid tid2
  const int lane2 = tid2 & 63;
#define lane lane2
  const int g2 = lane2 >> 4, n2 = lane2 & 15;
#define g g2
#define n n2
  const int tb = tid2 >> 7, tk = 4 * (tid2 & 127);
  if (n < 4) {
#pragma unroll
    for (int d = 0; d < 8; ++d) *(LAS f32x4*)(L + L_MRG + w * MRG_W + n * MRG_H + d * 64 + g * 16) = oa[d];
  }
  bar();
  {
    const f32x4 f0 = *(LAS f32x4*)(L + M_FW + tb * 32), f1 = *(LAS f32x4*)(L + M_FW + tb * 32 + 16);
    float o = 0.f;
#pragma unroll
    for (int ww = 0; ww < 8; ++ww) o += (ww < 4 ? f0[ww & 3] : f1[ww & 3]) * *(LAS float*)(L + L_MRG + ww * MRG_W + tb * MRG_H + (tid & 127) * 4);
    const float on = dpp<XOR1>(o);
    if ((tid & 1) == 0) __builtin_amdgcn_raw_buffer_store_b32(nosentu(pk2(o, on)), rws, WS_PART + cu * 1024 + tid * 2, 0, AUX_SC1);
  }
  STAMP(11);
  if (!(roleB || roleC)) { STAMP(16); DUMP(); return; }
  v4u wdr[4], gt[2];
  v2u xr;
  if (roleC) {
#pragma unroll
    for (int e = 0; e < 2; ++e) gt[e] = LD_SC1(WS_GATE + ((2 * w + e) * 64 + lane) * 16);
    xr = *(const GAS v2u*)((const GAS unsigned char*)p.x + tid * 8);
    const auto rd = RSRC(p.wd, 1792 * 512 * 2);
    const unsigned o = ((32 * (7 * X + (J - J_C)) + (lane >> 3)) * 512 + 64 * w + 8 * img_chunk(lane, lane >> 3)) * 2;
#pragma unroll
    for (int e = 0; e < 4; ++e) wdr[e] = __builtin_amdgcn_raw_buffer_load_b128(rd, o, e * 8 * 512 * 2, AUX_NT);
  }
  if (roleB) {
#pragma unroll
    for (int e = 0; e < 8; ++e) *(LAS v4u*)(L + L_W + w * 8192 + e * 1024 + lane * 16) = wr[e];
  } else {
    const int rw = lane >> 2, x = (rw >> 1) & 3, c = lane & 3;
    lptr pg = L + L_W + w * 8192 + rw * 128 + ((((c >> 1) ^ x) << 1) | (c & 1)) * 16, pu = L + L_W + w * 8192 + rw * 128 + ((((2 + (c >> 1)) ^ x) << 1) | (c & 1)) * 16;
#pragma unroll
    for (int e = 0; e < 4; ++e) { *(LAS v4u*)(pg + e * 2048) = wr[e]; *(LAS v4u*)(pu + e * 2048) = wr[4 + e]; }
  }
  if (roleB) {
    const int bb = w >> 1, hf = lane >> 5;
    v2u st[2];
    for (unsigned spin = 0; spin < SPIN_MAX; ++spin) {
#pragma unroll
      for (int hh = 0; hh < 2; ++hh) st[hh] = __builtin_amdgcn_raw_buffer_load_b64(rws, WS_STAT + ((8 * bb + 2 * (lane & 3) + hf) * 8 + X) * 32 + (2 * (w & 1) + hh) * 8, 0, AUX_SC1);
      if (all_lanes(max(max(st[0].x, st[0].y), max(st[1].x, st[1].y)) != SENT)) break;
      asm volatile("" ::: "memory");
    }
    float wt[2][4];
#pragma unroll
    for (int hh = 0; hh < 2; ++hh) {
      const float ms = __uint_as_float(st[hh].x);
      float M = fmaxf(ms, dpp<XOR1>(ms)); M = fmaxf(M, dpp<XOR2>(M)); M = x32_max(M);
      const float f = __builtin_amdgcn_exp2f((ms - M) * LOG2E);
      float Ls = f * __uint_as_float(st[hh].y); Ls += dpp<XOR1>(Ls); Ls += dpp<XOR2>(Ls); Ls = x32_sum(Ls);
      const float x = f * fast_rcp(Ls);
      wt[hh][0] = dpp<0x00>(x); wt[hh][1] = dpp<0x55>(x); wt[hh][2] = dpp<0xAA>(x); wt[hh][3] = dpp<0xFF>(x);
    }
    asm volatile("s_waitcnt lgkmcnt(0)" ::: "memory");
    v4u bo[2][4];
#pragma unroll
    for (int ct = 0; ct < 4; ++ct) { lptr wp = L + L_W + w * 8192 + frag_base(lane, ct); bo[0][ct] = w_frag(wp, 0); bo[1][ct] = w_frag(wp, 1); }
    int o_act = L_ACT + bb * RS + act_off(256 * (w & 1) + 4 * (lane & 31));
    int o_a = L_ACT + (lane & 3) * RS + g * 16 + (2 * w) * 64;
    int o_red = L_RED + w * 1024 + g * 256 + n * 4, o_rd = L_RED + lane * 16;
    int o_src = WS_PART + ((8 * bb + hf) * 8 + X) * 1024 + (w & 1) * 512 + (lane & 31) * 8;
    int o_dst = WS_ATT + w * 32768 + ((X * 4 + g) * 512 + 64 * (J - J_B) + 4 * n) * 2;
    PIN(o_act); PIN(o_a); PIN(o_red); PIN(o_rd); PIN(o_src); PIN(o_dst);
    STAMP(19);
    float o[2][4];
    {
      v2u po[8];
      GATHER8(8, po, o_src + (i & 3) * 2 * 8 * 1024 + (i >> 2) * 256);
#pragma unroll
      for (int hh = 0; hh < 2; ++hh) {
        o[hh][0] = x32_sum(wt[hh][0] * bf_lo(po[4 * hh].x) + wt[hh][1] * bf_lo(po[4 * hh + 1].x) + wt[hh][2] * bf_lo(po[4 * hh + 2].x) + wt[hh][3] * bf_lo(po[4 * hh + 3].x));
        o[hh][1] = x32_sum(wt[hh][0] * bf_hi(po[4 * hh].x) + wt[hh][1] * bf_hi(po[4 * hh + 1].x) + wt[hh][2] * bf_hi(po[4 * hh + 2].x) + wt[hh][3] * bf_hi(po[4 * hh + 3].x));
        o[hh][2] = x32_sum(wt[hh][0] * bf_lo(po[4 * hh].y) + wt[hh][1] * bf_lo(po[4 * hh + 1].y) + wt[hh][2] * bf_lo(po[4 * hh + 2].y) + wt[hh][3] * bf_lo(po[4 * hh + 3].y));
        o[hh][3] = x32_sum(wt[hh][0] * bf_hi(po[4 * hh].y) + wt[hh][1] * bf_hi(po[4 * hh + 1].y) + wt[hh][2] * bf_hi(po[4 * hh + 2].y) + wt[hh][3] * bf_hi(po[4 * hh + 3].y));
      }
    }
    STAMP(12);
    if (lane < 32) {
      *(LAS v2u*)(L + o_act) = v2u{pk2(o[0][0], o[0][1]), pk2(o[0][2], o[0][3])};
      *(LAS v2u*)(L + o_act + 256) = v2u{pk2(o[1][0], o[1][1]), pk2(o[1][2], o[1][3])};
    }
    bar();
    {
      const v4u a0 = lds128(L + o_a), a1 = lds128(L + o_a + 64);
#pragma unroll
      for (int ct = 0; ct < 4; ++ct) {
        f32x4 d = mfma(a0, bo[0][ct], f32x4{0.f, 0.f, 0.f, 0.f});
        d = mfma(a1, bo[1][ct], d);
        *(LAS float*)(L + o_red + ct * 64) = sel4(d, g);
      }
    }
    bar();
    {
      f32x4 s0 = *(LAS f32x4*)(L + o_rd), s1 = *(LAS f32x4*)(L + o_rd + 1024);
#pragma unroll
      for (int ww = 2; ww < 8; ww += 2) { s0 += *(LAS f32x4*)(L + o_rd + ww * 1024); s1 += *(LAS f32x4*)(L + o_rd + ww * 1024 + 1024); }
      s0 += s1;
      __builtin_amdgcn_raw_buffer_store_b64((v2u{nosentu(pk2(s0.x, s0.y)), nosentu(pk2(s0.z, s0.w))}), rws, o_dst, 0, AUX_SC1);
    }
    STAMP(13); STAMP(16); DUMP();
    return;
  }
  const int pc = 7 * X + (J - J_C);
#pragma unroll
  for (int e = 0; e < 4; ++e) *(LAS v4u*)(L + L_WD + w * 4096 + e * 1024 + lane * 16) = wdr[e];
  if (!all_lanes(umax4(gt[0]) != SENT && umax4(gt[1]) != SENT)) {
    for (unsigned spin = 0; spin < SPIN_MAX; ++spin) {
      asm volatile("" ::: "memory");
#pragma unroll
      for (int e = 0; e < 2; ++e) gt[e] = LD_SC1(WS_GATE + ((2 * w + e) * 64 + lane) * 16);
      if (all_lanes(umax4(gt[0]) != SENT && umax4(gt[1]) != SENT)) break;
    }
  }
#pragma unroll
  for (int e = 0; e < 2; ++e) *(LAS v4u*)(L + L_GATE + ((2 * w + e) * 64 + lane) * 16) = gt[e];
  if (tid < 4) *(LAS v4u*)(L + M_ZERO + tid * 16) = v4u{0u, 0u, 0u, 0u};
  bar();
  {
    const v2u ga = *(LAS v2u*)(L + L_GATE + tb * 4096 + tk * 2), sb = *(LAS v2u*)(L + L_GATE + tb * 4096 + 1024 + tk * 2), cb = *(LAS v2u*)(L + L_GATE + tb * 4096 + 2048 + tk * 2);
    const v2u xw = xr;
    const float gav[4] = {bf_lo(ga.x), bf_hi(ga.x), bf_lo(ga.y), bf_hi(ga.y)}, sbv[4] = {bf_lo(sb.x), bf_hi(sb.x), bf_lo(sb.y), bf_hi(sb.y)},
                c1v[4] = {rb(1.f + bf_lo(cb.x)), rb(1.f + bf_hi(cb.x)), rb(1.f + bf_lo(cb.y)), rb(1.f + bf_hi(cb.y))}, xs[4] = {bf_lo(xw.x), bf_hi(xw.x), bf_lo(xw.y), bf_hi(xw.y)};
    v4u bg[2][4], bd[4];
#pragma unroll
    for (int ct = 0; ct < 4; ++ct) { lptr wp = L + L_W + w * 8192 + frag_base(lane, ct); bg[0][ct] = w_frag(wp, 0); bg[1][ct] = w_frag(wp, 1); }
    {
      const int q = (lane >> 2) & 3;
#pragma unroll
      for (int kp = 0; kp < 4; ++kp) {
        const int ra = 8 * kp + q, rb_ = ra + 4;
        bd[kp] = tr_frag(L + L_WD + w * 4096 + ra * 128 + ((g ^ ((ra >> 1) & 3)) * 32) + (lane & 3) * 8, L + L_WD + w * 4096 + rb_ * 128 + ((g ^ ((rb_ >> 1) & 3)) * 32) + (lane & 3) * 8);
      }
    }
    int o_src = WS_ATT + X * 32768 + (tb * 512 + tk) * 2;
    int o_x = L_X + tid * 8, o_ln = M_LN + tb * 16, o_act = L_ACT + tb * RS + act_off(tk), o_a = L_ACT + (lane & 3) * RS + g * 16 + (2 * w) * 64;
    int o_red = L_RED + w * 1024 + g * 256 + n * 4, o_rd = L_RED + (2 * w + (lane >> 5)) * 256 + (lane & 31) * 4;
    int o_hw = M_HID + (2 * w + (lane >> 5)) * 64 + (lane & 31) * 2, o_hr = (n >> 2) == g ? M_HID + (n & 3) * 64 : M_ZERO;
    int o_dst = WS_DOWN + pc * 8192 + tid * 16;
    PIN(o_src); PIN(o_x); PIN(o_ln); PIN(o_act); PIN(o_a); PIN(o_red); PIN(o_rd); PIN(o_hw); PIN(o_hr); PIN(o_dst);
    float gv[4] = {gav[0], gav[1], gav[2], gav[3]}, sv[4] = {sbv[0], sbv[1], sbv[2], sbv[3]}, cv1[4] = {c1v[0], c1v[1], c1v[2], c1v[3]}, xx[4] = {xs[0], xs[1], xs[2], xs[3]};
#pragma unroll
    for (int e = 0; e < 4; ++e) { PIN(gv[e]); PIN(sv[e]); PIN(cv1[e]); PIN(xx[e]); }
    STAMP(19);
    float at[4];
    {
      v2u av[8];
      GATHER8(8, av, o_src + i * 4 * 1024);
      at[0] = ((bf_lo(av[0].x) + bf_lo(av[1].x)) + (bf_lo(av[2].x) + bf_lo(av[3].x))) + ((bf_lo(av[4].x) + bf_lo(av[5].x)) + (bf_lo(av[6].x) + bf_lo(av[7].x)));
      at[1] = ((bf_hi(av[0].x) + bf_hi(av[1].x)) + (bf_hi(av[2].x) + bf_hi(av[3].x))) + ((bf_hi(av[4].x) + bf_hi(av[5].x)) + (bf_hi(av[6].x) + bf_hi(av[7].x)));
      at[2] = ((bf_lo(av[0].y) + bf_lo(av[1].y)) + (bf_lo(av[2].y) + bf_lo(av[3].y))) + ((bf_lo(av[4].y) + bf_lo(av[5].y)) + (bf_lo(av[6].y) + bf_lo(av[7].y)));
      at[3] = ((bf_hi(av[0].y) + bf_hi(av[1].y)) + (bf_hi(av[2].y) + bf_hi(av[3].y))) + ((bf_hi(av[4].y) + bf_hi(av[5].y)) + (bf_hi(av[6].y) + bf_hi(av[7].y)));
    }
    STAMP(14);
    unsigned xp[2];
    float x2[4];
#pragma unroll
    for (int e = 0; e < 4; e += 2) {
      const unsigned a = pk2(at[e], at[e + 1]);
      const unsigned t = pk2(gv[e] * bf_lo(a), gv[e + 1] * bf_hi(a));
      xp[e >> 1] = pk2(xx[e] + bf_lo(t), xx[e + 1] + bf_hi(t));
      x2[e] = bf_lo(xp[e >> 1]); x2[e + 1] = bf_hi(xp[e >> 1]);
    }
    float s1 = (x2[0] + x2[1]) + (x2[2] + x2[3]), s2 = (x2[0] * x2[0] + x2[1] * x2[1]) + (x2[2] * x2[2] + x2[3] * x2[3]);
    wave_sum2(s1, s2);
    if (lane == 0) *(LAS v2u*)(L + M_LN + w * 8) = v2u{__float_as_uint(s1), __float_as_uint(s2)};
    *(LAS v2u*)(L + o_x) = v2u{xp[0], xp[1]};
    bar();
    STAMP(20);
    {
      const f32x4 s = *(LAS f32x4*)(L + o_ln);
      float mean_r, rden;
      ln_consts(s.x + s.z, s.y + s.w, mean_r, rden);
      unsigned hp[2];
#pragma unroll
      for (int e = 0; e < 4; e += 2) {
        const unsigned a = pk2(x2[e] - mean_r, x2[e + 1] - mean_r);
        const unsigned t = pk2(bf_lo(a) * rden, bf_hi(a) * rden);
        const unsigned u = pk2(bf_lo(t) * cv1[e], bf_hi(t) * cv1[e + 1]);
        hp[e >> 1] = pk2(bf_lo(u) + sv[e], bf_hi(u) + sv[e + 1]);
      }
      *(LAS v2u*)(L + o_act) = v2u{hp[0], hp[1]};
    }
    bar();
    STAMP(21);
    {
      const v4u a0 = lds128(L + o_a), a1 = lds128(L + o_a + 64);
#pragma unroll
      for (int ct = 0; ct < 4; ++ct) {
        f32x4 d = mfma(a0, bg[0][ct], f32x4{0.f, 0.f, 0.f, 0.f});
        d = mfma(a1, bg[1][ct], d);
        *(LAS float*)(L + o_red + ct * 64) = sel4(d, g);
      }
    }
    bar();
    STAMP(22);
    if (w < 2) {
      float p0[8], p1[8];
#pragma unroll
      for (int ww = 0; ww < 8; ++ww) { p0[ww] = *(LAS float*)(L + o_rd + ww * 1024); p1[ww] = *(LAS float*)(L + o_rd + ww * 1024 + 128); }
      const unsigned gu = pk2(((p0[0] + p0[1]) + (p0[2] + p0[3])) + ((p0[4] + p0[5]) + (p0[6] + p0[7])), ((p1[0] + p1[1]) + (p1[2] + p1[3])) + ((p1[4] + p1[5]) + (p1[6] + p1[7])));
      const float sg = bf_lo(gu), up = bf_hi(gu);
      const float u = sg * (1.f + 0.044715f * sg * sg) * (-2.f * LOG2E * 0.7978845608028654f);
      const float ge = rb(sg * fast_rcp(1.f + __builtin_amdgcn_exp2f(u)));
      *(LAS unsigned short*)(L + o_hw) = (unsigned short)(pk2(ge * up, 0.f) & 0xffffu);
    }
    bar();
    STAMP(23);
    {
      const v4u h0 = lds128(L + o_hr), h1 = lds128(L + o_hr + 16), h2 = lds128(L + o_hr + 32), h3 = lds128(L + o_hr + 48);
      const f32x4 z = {0.f, 0.f, 0.f, 0.f};
      const f32x4 d = mfma(h1, bd[1], mfma(h0, bd[0], z)) + mfma(h3, bd[3], mfma(h2, bd[2], z));
      ST_SC1((v4u{nosent(d[0]), nosent(d[1]), nosent(d[2]), nosent(d[3])}), o_dst);
    }
  }
  STAMP(15);
  if (J < J_F_END) {
    const int f = 4 * X + (J - J_C);
    float gb = bf_lo(*(LAS unsigned short*)(L + L_GATE + g * 4096 + 3072 + (16 * f + n) * 2));
    float x2 = bf_lo(*(LAS unsigned short*)(L + L_X + (g * 512 + 16 * f + n) * 2));
    int o_src = WS_DOWN + (4 * w + g) * 8192 + (16 * f + n) * 16, o_wr = L_RED + w * 256 + lane * 16, o_rd = L_RED + n * 16 + g * 4, o_out = (g * 512 + 16 * f + n) * 2;
    PIN(gb); PIN(x2); PIN(o_src); PIN(o_wr); PIN(o_rd); PIN(o_out);
    if (w < 7) {
      f32x4 s;
      v4u dv[2];
      GATHER(2, dv, o_src + i * 28 * 8192);
#pragma unroll
      for (int b = 0; b < 4; ++b) s[b] = x32_sum(x16_sum(__uint_as_float(dv[0][b]) + __uint_as_float(dv[1][b])));
      if (lane < 16) *(LAS f32x4*)(L + o_wr) = s;
    }
    bar();
    if (w == 0) {
      float q7[7];
#pragma unroll
      for (int ww = 0; ww < 7; ++ww) q7[ww] = *(LAS float*)(L + o_rd + ww * 256);
      const float y = rb(((q7[0] + q7[1]) + (q7[2] + q7[3])) + ((q7[4] + q7[5]) + q7[6]));
      const unsigned ob = pk2(x2 + rb(gb * y), 0.f) & 0xffffu;
      const unsigned o2 = ob | (dppu<XOR1>(ob) << 16);
      if ((n & 1) == 0) *(GAS unsigned*)((GAS unsigned char*)p.out + o_out) = o2;
    }
  }
  STAMP(16); DUMP();
#undef tid
#undef lane
#undef g
#undef n
}
}

extern "C" void kernel_launch(void* const* d_in, const int* in_sizes, int n_in, void* d_out, int out_size, void* d_ws, size_t ws_size, hipStream_t stream) {
  (void)in_sizes; (void)n_in; (void)out_size; (void)ws_size;
  Params p;
  p.x = d_in[0]; p.k = d_in[1]; p.v = d_in[2]; p.wq = d_in[3]; p.wo = d_in[4];
  p.wg = d_in[6]; p.wu = d_in[7]; p.wd = d_in[8]; p.c = d_in[9]; p.wm = d_in[10];
  p.out = d_out; p.ws = d_ws;
  hipLaunchKernelGGL(adaln_block_kernel, dim3(NCU), dim3(NTHR), 0, stream, p);
}
```

```cpp
#include <hip/hip_runtime.h>
#include <hip/hip_bf16.h>
#include <stdint.h>

#define GAS __attribute__((address_space(1)))
#define LAS __attribute__((address_space(3)))
typedef unsigned v4u __attribute__((ext_vector_type(4)));
typedef unsigned v2u __attribute__((ext_vector_type(2)));
typedef float f32x4 __attribute__((ext_vector_type(4)));
typedef short s16x4 __attribute__((ext_vector_type(4)));
typedef __bf16 bf16x8 __attribute__((ext_vector_type(8)));
typedef __bf16 bf16x2 __attribute__((ext_vector_type(2)));
typedef LAS unsigned char* lptr;

namespace {
constexpr int NCU = 256, NTHR = 512;
constexpr unsigned KV_BYTES = 4u * 8u * 8192u * 128u * 2u;
constexpr unsigned WS_READY = 4096,
                    WS_H = 8192, WS_MP = 16384,
                   WS_QP = WS_MP + 48 * 4 * 2048,
                   WS_GATE = WS_QP + 8 * 4 * 4 * 2048,
                   WS_STAT = WS_GATE + 16384, WS_ATT = WS_STAT + 8192,
                   WS_PART = WS_ATT + 8 * 32768,
                   WS_DOWN = WS_PART + 256 * 1024, WS_END = WS_DOWN + 56 * 8192;
constexpr int RS = 1088;
constexpr int L_WM = 0, L_WQ = 16384, L_WD = 0, L_W = 32768, L_MRG = 98304, L_GATE = L_MRG + 18432, L_ACT = L_GATE + 16384, L_ACTH = L_ACT + 4352, L_X = L_ACTH + 4352,
              L_Q = L_X + 4096, L_RED = L_Q + 1024, L_MISC = L_RED + 8192, L_BYTES = L_MISC + 1024;
constexpr int MRG_H = 576, MRG_W = 4 * MRG_H;
constexpr int M_LN = L_MISC, M_MX = L_MISC + 128, M_LS = L_MISC + 256, M_HID = L_MISC + 384, M_CLR = L_MISC + 644, M_CNT = L_MISC + 648, M_CN2 = L_MISC + 652, M_FW = L_MISC + 768, M_ZERO = L_MISC + 896;
constexpr unsigned SENT = 0xFFFFFFFFu;
constexpr unsigned SPIN_MAX = 1u << 17;
constexpr unsigned SPIN_LDS = 1u << 22;
constexpr int PACE = 5;
constexpr int AUX_NT = 2, AUX_SC1 = 16;
constexpr float LOG2E = 1.4426950408889634f;
constexpr int J_G = 8, J_H = 24, J_GR_END = 28;
constexpr int J_B = 8, J_C = 16, J_F_END = 20, J_C_END = 23;

struct Params { const void* x; const void* k; const void* v; const void* wq; const void* wo; const void* wg; const void* wu; const void* wd; const void* c; const void* wm; void* out; void* ws; };

__device__ __forceinline__ float bf_lo(unsigned u) { return __builtin_bit_cast(float, u << 16); }
__device__ __forceinline__ float bf_hi(unsigned u) { return __builtin_bit_cast(float, u & 0xffff0000u); }
__device__ __forceinline__ unsigned pk2(float a, float b) { bf16x2 r = {(__bf16)a, (__bf16)b}; return __builtin_bit_cast(unsigned, r); }
__device__ __forceinline__ float rb(float a) { return bf_lo(pk2(a, a)); }
template <int CTRL> __device__ __forceinline__ float dpp(float x) { return __builtin_bit_cast(float, __builtin_amdgcn_mov_dpp(__builtin_bit_cast(int, x), CTRL, 0xf, 0xf, true)); }
template <int CTRL> __device__ __forceinline__ unsigned dppu(unsigned x) { return (unsigned)__builtin_amdgcn_mov_dpp((int)x, CTRL, 0xf, 0xf, true); }
constexpr int XOR1 = 0xB1, XOR2 = 0x4E, HMIR = 0x141, ROR4 = 0x124, ROR8 = 0x128;
__device__ __forceinline__ float x16_sum(float x) { auto s = __builtin_amdgcn_permlane16_swap(__float_as_uint(x), __float_as_uint(x), false, false); return __uint_as_float(s[0]) + __uint_as_float(s[1]); }
__device__ __forceinline__ float x32_sum(float x) { auto s = __builtin_amdgcn_permlane32_swap(__float_as_uint(x), __float_as_uint(x), false, false); return __uint_as_float(s[0]) + __uint_as_float(s[1]); }
__device__ __forceinline__ float x16_max(float x) { auto s = __builtin_amdgcn_permlane16_swap(__float_as_uint(x), __float_as_uint(x), false, false); return fmaxf(__uint_as_float(s[0]), __uint_as_float(s[1])); }
__device__ __forceinline__ float x32_max(float x) { auto s = __builtin_amdgcn_permlane32_swap(__float_as_uint(x), __float_as_uint(x), false, false); return fmaxf(__uint_as_float(s[0]), __uint_as_float(s[1])); }
__device__ __forceinline__ float row_sum(float x) { x += dpp<XOR1>(x); x += dpp<XOR2>(x); x += dpp<HMIR>(x); x += dpp<ROR8>(x); return x; }
__device__ __forceinline__ void wave_sum2(float& x, float& y) {
  x += dpp<XOR1>(x); y += dpp<XOR1>(y); x += dpp<XOR2>(x); y += dpp<XOR2>(y); x += dpp<HMIR>(x); y += dpp<HMIR>(y); x += dpp<ROR8>(x); y += dpp<ROR8>(y);
  x = x16_sum(x); y = x16_sum(y); x = x32_sum(x); y = x32_sum(y);
}
__device__ __forceinline__ f32x4 mfma(v4u a, v4u b, f32x4 c) { return __builtin_amdgcn_mfma_f32_16x16x32_bf16(__builtin_bit_cast(bf16x8, a), __builtin_bit_cast(bf16x8, b), c, 0, 0, 0); }
__device__ __forceinline__ v2u tr_read(lptr p) { s16x4 r = __builtin_amdgcn_ds_read_tr16_b64_v4i16((LAS s16x4*)p); return __builtin_bit_cast(v2u, r); }
__device__ __forceinline__ v4u tr_frag(lptr p0, lptr p1) { v2u a = tr_read(p0), b = tr_read(p1); return v4u{a.x, a.y, b.x, b.y}; }
__device__ __forceinline__ v4u lds128(lptr p) { return *(LAS v4u*)p; }
__device__ __forceinline__ void bar() { asm volatile("s_waitcnt lgkmcnt(0)\n\ts_barrier" ::: "memory"); }
__device__ __forceinline__ void glds16(const void* gsrc, unsigned lds_dst) {
  unsigned keep;
  lds_dst = __builtin_amdgcn_readfirstlane(lds_dst);
  asm volatile("s_mov_b32 %0, m0\n\ts_mov_b32 m0, %2\n\ts_nop 0\n\tglobal_load_lds_dwordx4 %1, off nt\n\ts_mov_b32 m0, %0" : "=&s"(keep) : "v"(gsrc), "s"(lds_dst) : "memory");
}
__device__ __forceinline__ unsigned nosent(float f) { return min(__float_as_uint(f), SENT - 1u); }
__device__ __forceinline__ unsigned nosentu(unsigned u) { return min(u, SENT - 1u); }
__device__ __forceinline__ unsigned umax4(v4u a) { return max(max(a.x, a.y), max(a.z, a.w)); }
__device__ __forceinline__ bool all_lanes(bool ok) { return __builtin_amdgcn_ballot_w64(!ok) == 0ull; }
__device__ __forceinline__ float sel4(f32x4 d, int g) { const bool o = (g & 1) != 0; const float a = o ? d[1] : d[0], b = o ? d[3] : d[2]; return (g & 2) ? b : a; }
#define PIN(x) asm volatile("" : "+v"(x))
__device__ __forceinline__ float fast_rcp(float x) { return __builtin_amdgcn_rcpf(x); }
#define RSRC(p, n) __builtin_amdgcn_make_buffer_rsrc((void*)(p), 0, (int)(n), 0x00020000)
#define LD_SC1(off) __builtin_amdgcn_raw_buffer_load_b128(rws, (off), 0, AUX_SC1)
#define ST_SC1(v, off) __builtin_amdgcn_raw_buffer_store_b128((v), rws, (off), 0, AUX_SC1)
#define FENCE() __builtin_amdgcn_sched_barrier(0)
#define LD8_SC1(off) __builtin_amdgcn_raw_buffer_load_b64(rws, (off), 0, AUX_SC1)
#define GATHER8(N, V, OFF) do { \
    unsigned pend_ = 0; \
    _Pragma("unroll") for (int i = 0; i < (N); ++i) V[i] = LD8_SC1(OFF); \
    _Pragma("unroll") for (int i = 0; i < (N); ++i) pend_ |= all_lanes(max(V[i].x, V[i].y) != SENT) ? 0u : 1u << i; \
    for (unsigned spin_ = 0; pend_ != 0u && spin_ < SPIN_MAX; ++spin_) { \
      asm volatile("" ::: "memory"); \
      _Pragma("unroll") for (int i = 0; i < (N); ++i) if (pend_ & (1u << i)) V[i] = LD8_SC1(OFF); \
      _Pragma("unroll") for (int i = 0; i < (N); ++i) if (pend_ & (1u << i)) { if (all_lanes(max(V[i].x, V[i].y) != SENT)) pend_ &= ~(1u << i); } \
    } } while (0)
#define GATHER(N, V, OFF) do { \
    unsigned pend_ = 0; \
    _Pragma("unroll") for (int i = 0; i < (N); ++i) V[i] = LD_SC1(OFF); \
    _Pragma("unroll") for (int i = 0; i < (N); ++i) pend_ |= all_lanes(umax4(V[i]) != SENT) ? 0u : 1u << i; \
    for (unsigned spin_ = 0; pend_ != 0u && spin_ < SPIN_MAX; ++spin_) { \
      asm volatile("" ::: "memory"); \
      _Pragma("unroll") for (int i = 0; i < (N); ++i) if (pend_ & (1u << i)) V[i] = LD_SC1(OFF); \
      _Pragma("unroll") for (int i = 0; i < (N); ++i) if (pend_ & (1u << i)) { if (all_lanes(umax4(V[i]) != SENT)) pend_ &= ~(1u << i); } \
    } } while (0)

__device__ __forceinline__ int img_chunk(int lane, int row) { return ((((lane & 7) >> 1) ^ ((row >> 1) & 3)) << 1) | (lane & 1); }
__device__ __forceinline__ int frag_base(int lane, int ct) { const int g = lane >> 4, q = (lane >> 2) & 3; return (4 * g + q) * 128 + ((ct ^ ((2 * g + (q >> 1)) & 3)) * 32) + (lane & 3) * 8; }
__device__ __forceinline__ v4u w_frag(lptr base, int s) { return tr_frag(base + s * 4096, base + s * 4096 + 2048); }
__device__ __forceinline__ int act_off(int k) { return (k >> 5) * 64 + ((k >> 2) & 3) * 16 + ((k >> 4) & 1) * 8; }
__device__ __forceinline__ void ln_consts(float sum, float sumsq, float& mean_r, float& rden) {
  const float mean = sum * (1.f / 512.f);
  const float var = fmaxf(sumsq * (1.f / 512.f) - mean * mean, 0.f);
  mean_r = rb(mean);
  rden = fast_rcp(rb(__builtin_amdgcn_sqrtf(rb(rb(var) + 1e-5f))));
}
__device__ __forceinline__ float ln_mod(float x, float mean_r, float rden, float c1, float ss) {
  return rb(rb(rb(rb(x - mean_r) * rden) * c1) + ss);
}

__global__ void __launch_bounds__(NTHR, 2) adaln_block_kernel(Params p) {
  __shared__ __attribute__((aligned(16))) unsigned char smem[L_BYTES];
  lptr L = (lptr)smem;
  const unsigned lds0 = (unsigned)(uintptr_t)L;
  const int tid = threadIdx.x, lane = tid & 63, w = __builtin_amdgcn_readfirstlane(tid >> 6);
  const int cu = blockIdx.x, X = cu & 7, J = cu >> 3;
  const int g = lane >> 4, n = lane & 15;
  const auto rws = RSRC(p.ws, WS_END);
  const auto rk = RSRC(p.k, KV_BYTES);
  const auto rv = RSRC(p.v, KV_BYTES);
  const bool roleM = J < J_H, roleH = J >= J_H, roleGR = J >= J_H && J < J_GR_END, roleB = J >= J_B && J < J_C, roleC = J >= J_C && J < J_C_END;
  const int kq = J & 3;
  const int mg = J < J_G ? 2 * X + (J >> 2) : 16 + 4 * X + ((J - J_G) >> 2);
  {
    const int r0 = 16 * w + (lane >> 3), ch = img_chunk(lane, r0);
    if (roleM) {
      const GAS unsigned char* s = (const GAS unsigned char*)p.wm + ((size_t)(128 * kq + r0) * 3072 + 64 * mg + 8 * ch) * 2;
      glds16((const void*)s, lds0 + L_WM + w * 2048);
      glds16((const void*)(s + 8 * 3072 * 2), lds0 + L_WM + w * 2048 + 1024);
    }
    const GAS unsigned char* s = (const GAS unsigned char*)p.wq + ((size_t)(128 * kq + r0) * 4096 + 64 * (8 * X + (J >> 2)) + 8 * ch) * 2;
    glds16((const void*)s, lds0 + L_WQ + w * 2048);
    glds16((const void*)(s + 8 * 4096 * 2), lds0 + L_WQ + w * 2048 + 1024);
  }
  const unsigned long long ep = __hip_atomic_load((GAS unsigned long long*)p.ws, __ATOMIC_RELAXED, __HIP_MEMORY_SCOPE_AGENT);
  v2u cv = {0u, 0u};
  if (roleM && tid < 128) cv = *(const GAS v2u*)((const GAS unsigned char*)p.c + ((tid >> 5) * 512 + 128 * kq + 4 * (tid & 31)) * 2);
  asm volatile("s_barrier" ::: "memory");
  if (w == 1) {
    const v4u sent = {SENT, SENT, SENT, SENT};
    ST_SC1(sent, WS_QP + ((X * 4 + g) * 4 + kq) * 2048 + (J >> 2) * 256 + n * 16);
    ST_SC1(sent, WS_PART + cu * 1024 + lane * 16);
    if (lane < 2) ST_SC1(sent, WS_STAT + cu * 32 + lane * 16);
    if (roleB) {
#pragma unroll
      for (int e = 0; e < 4; ++e) ST_SC1(sent, WS_ATT + (2 * e + (lane >> 5)) * 32768 + ((X * 4 + ((lane >> 3) & 3)) * 512 + 64 * (J - J_B)) * 2 + (lane & 7) * 16);
    }
    if (roleGR && n < 8) ST_SC1(sent, WS_GATE + g * 4096 + (4 * X + (J - J_H)) * 128 + n * 16);
    if (roleC) {
#pragma unroll
      for (int e = 0; e < 8; ++e) ST_SC1(sent, WS_DOWN + (7 * X + (J - J_C)) * 8192 + e * 1024 + lane * 16);
    }
  }
  FENCE();
  const bool roleT = roleB || roleC;
  const unsigned kvbase = (((unsigned)(J >> 3) * 8u + (unsigned)X) * 8192u + (unsigned)(J & 7) * 1024u) * 256u + (unsigned)w * 4096u;
  const unsigned koff = (4 * (lane & 3) + ((lane >> 2) & 1)) * 256 + (lane >> 3) * 16;
  v4u kf[8][4];
#define K_ISSUE(t) do { unsigned ko_ = koff; asm volatile("" : "+v"(ko_)); _Pragma("unroll") for (int q_ = 0; q_ < 4; ++q_) kf[t][q_] = __builtin_amdgcn_raw_buffer_load_b128(rk, ko_ + (q_ >> 1) * 512 + (q_ & 1) * 128, kvbase + (t) * 32768, AUX_NT); FENCE(); } while (0)
  K_ISSUE(0);
  if (w == 2) {
    const GAS unsigned char* a;
    if (roleB) a = (const GAS unsigned char*)p.wo + (size_t)(512 * X + 8 * lane) * 1024;
    else if (roleC) a = (const GAS unsigned char*)(lane < 32 ? p.wg : p.wu) + (size_t)(lane & 31) * (16 * 3584);
    else a = (const GAS unsigned char*)p.v + kvbase + (lane & 7) * 32768;
    glds16((const void*)a, lds0 + L_RED + 4096);
    if (roleC) {
      const GAS unsigned char* c2 = lane < 32 ? (const GAS unsigned char*)p.wd + (size_t)(32 * (7 * X + (J - J_C)) + lane) * 1024 : lane < 48 ? (const GAS unsigned char*)p.out + (lane & 3) * 1024 : (const GAS unsigned char*)p.x + (lane & 3) * 1024;
      glds16((const void*)c2, lds0 + L_RED + 5120);
    }
  }
  if (tid == 0) { *(LAS unsigned*)(L + M_CLR) = 0u; *(LAS unsigned*)(L + M_CNT) = 0u; *(LAS unsigned*)(L + M_CN2) = 0u; }
  const unsigned long long E = ep + 1ull;
  const unsigned ta = __builtin_amdgcn_readfirstlane((unsigned)E), tb2 = __builtin_amdgcn_readfirstlane(((unsigned)E * 0x9E3779B1u) ^ (unsigned)(E >> 32));
  if (roleM && tid < 128) {
    float cs[4] = {bf_lo(cv.x), bf_hi(cv.x), bf_lo(cv.y), bf_hi(cv.y)};
#pragma unroll
    for (int e = 0; e < 4; ++e) cs[e] = cs[e] / (1.f + expf(-cs[e]));
    *(LAS v2u*)(L + L_ACT + (tid >> 5) * RS + act_off(4 * (tid & 31))) = v2u{pk2(cs[0], cs[1]), pk2(cs[2], cs[3])};
  }
  asm volatile("s_waitcnt vmcnt(4)" ::: "memory");
  if (w == 1) {
    if (lane == 0) ST_SC1((v4u{0u, ta, 0u, tb2}), WS_READY + cu * 16);
  }
  bar();
  if (w == 1 && lane == 0) *(volatile LAS unsigned*)(L + M_CLR) = 1u;
  if (roleM && w < 4) {
    lptr ap = L + L_ACT + (lane & 3) * RS + g * 16;
    lptr wp = L + L_WM + frag_base(lane, w);
    f32x4 a0 = mfma(lds128(ap), w_frag(wp, 0), f32x4{0.f, 0.f, 0.f, 0.f}), a1 = mfma(lds128(ap + 64), w_frag(wp, 1), f32x4{0.f, 0.f, 0.f, 0.f});
    a0 = mfma(lds128(ap + 128), w_frag(wp, 2), a0); a1 = mfma(lds128(ap + 192), w_frag(wp, 3), a1);
    const float v = sel4(a0 + a1, g);
    const float vn = dpp<XOR1>(v);
    if ((n & 1) == 0) ST_SC1((v4u{__float_as_uint(v), ta, __float_as_uint(vn), tb2}), WS_MP + ((mg * 4 + kq) * 4 + g) * 512 + (8 * w + (n >> 1)) * 16);
    FENCE();
  }
  if (roleH && w == 0) {
    __builtin_amdgcn_s_setprio(2);
    const int eta = 8 * X + (J - J_H), b = (lane >> 2) & 3, pr = lane & 3;
    v4u xq[4];
#pragma unroll
    for (int i = 0; i < 4; ++i) xq[i] = *(const GAS v4u*)((const GAS unsigned char*)p.x + (g * 512 + 32 * n + 8 * i) * 2);
    const unsigned xp = *(const GAS unsigned*)((const GAS unsigned char*)p.x + (b * 512 + 8 * eta + 2 * pr) * 2);
    float s1 = 0.f, s2 = 0.f;
#pragma unroll
    for (int i = 0; i < 4; ++i)
#pragma unroll
      for (int e = 0; e < 4; ++e) { const float a = bf_lo(xq[i][e]), c = bf_hi(xq[i][e]); s1 += a + c; s2 += a * a + c * c; }
    s1 = row_sum(s1); s2 = row_sum(s2);
    float mean_r, rden;
    ln_consts(s1, s2, mean_r, rden);
    mean_r = __builtin_bit_cast(float, __builtin_amdgcn_ds_bpermute(64 * b, __builtin_bit_cast(int, mean_r)));
    rden = __builtin_bit_cast(float, __builtin_amdgcn_ds_bpermute(64 * b, __builtin_bit_cast(int, rden)));
    v4u ms, mc;
    for (unsigned spin = 0; spin < SPIN_MAX; ++spin) {
      ms = LD_SC1(WS_MP + (((eta >> 3) * 4 + g) * 4 + b) * 512 + ((eta & 7) * 4 + pr) * 16);
      mc = LD_SC1(WS_MP + (((8 + (eta >> 3)) * 4 + g) * 4 + b) * 512 + ((eta & 7) * 4 + pr) * 16);
      if (all_lanes((ms.y == ta) & (ms.w == tb2) & (mc.y == ta) & (mc.w == tb2))) break;
      asm volatile("" ::: "memory");
    }
    const float sa0 = rb(x32_sum(x16_sum(__uint_as_float(ms.x)))), sa1 = rb(x32_sum(x16_sum(__uint_as_float(ms.z))));
    const float ca0 = rb(x32_sum(x16_sum(__uint_as_float(mc.x)))), ca1 = rb(x32_sum(x16_sum(__uint_as_float(mc.z))));
    const unsigned hp = pk2(ln_mod(bf_lo(xp), mean_r, rden, rb(1.f + ca0), sa0), ln_mod(bf_hi(xp), mean_r, rden, rb(1.f + ca1), sa1));
    const unsigned hn = dppu<XOR1>(hp);
    if (lane < 16 && (pr & 1) == 0) ST_SC1((v4u{hp, ta, hn, tb2}), WS_H + eta * 128 + (b * 2 + (pr >> 1)) * 16);
    __builtin_amdgcn_s_setprio(0);
    FENCE();
  }
#define PACE_WAITN(x) do { if (w != 0) asm volatile("s_waitcnt vmcnt(%0)" ::"i"(PACE + (x)) : "memory"); } while (0)
#define PACE_WAIT() PACE_WAITN(0)
  K_ISSUE(1);
  PACE_WAIT(); K_ISSUE(2); PACE_WAIT(); K_ISSUE(3);
  if (w == 0) {
    __builtin_amdgcn_s_setprio(2);
    v4u bq[4][4];
#pragma unroll
    for (int ct = 0; ct < 4; ++ct) { lptr wp = L + L_WQ + frag_base(lane, ct);
#pragma unroll
      for (int s = 0; s < 4; ++s) bq[ct][s] = w_frag(wp, s); }
    v4u hv[2];
    for (unsigned spin = 0; spin < SPIN_MAX; ++spin) {
      bool ok = true;
#pragma unroll
      for (int i = 0; i < 2; ++i) { hv[i] = LD_SC1(WS_H + (128 * kq + 64 * i + lane) * 16); ok &= (hv[i].y == ta) & (hv[i].w == tb2); }
#pragma unroll
      for (int i = 0; i < 4; ++i) { const v4u rd = LD_SC1(WS_READY + (64 * i + lane) * 16); ok &= (rd.y == ta) & (rd.w == tb2); }
      if (all_lanes(ok)) break;
      asm volatile("" ::: "memory");
    }
#pragma unroll
    for (int i = 0; i < 2; ++i) { const int gm = 64 * i + lane; *(LAS v2u*)(L + L_ACTH + ((gm >> 1) & 3) * RS + act_off(8 * (gm >> 3) + 4 * (gm & 1))) = v2u{hv[i].x, hv[i].z}; }
    asm volatile("s_waitcnt lgkmcnt(0)" ::: "memory");
    lptr ap = L + L_ACTH + (lane & 3) * RS + g * 16;
    const v4u a0 = lds128(ap), a1 = lds128(ap + 64), a2 = lds128(ap + 128), a3 = lds128(ap + 192);
    float qv[4];
#pragma unroll
    for (int ct = 0; ct < 4; ++ct) {
      const f32x4 z = {0.f, 0.f, 0.f, 0.f};
      qv[ct] = sel4(mfma(a1, bq[ct][1], mfma(a0, bq[ct][0], z)) + mfma(a3, bq[ct][3], mfma(a2, bq[ct][2], z)), g);
    }
    for (unsigned spin = 0; spin < SPIN_LDS; ++spin) { if (*(volatile LAS unsigned*)(L + M_CLR) != 0u) break; __builtin_amdgcn_s_sleep(1); }
    ST_SC1((v4u{nosent(qv[0]), nosent(qv[1]), nosent(qv[2]), nosent(qv[3])}), WS_QP + ((X * 4 + g) * 4 + kq) * 2048 + (J >> 2) * 256 + n * 16);
    if (cu == 0 && lane == 0) __hip_atomic_store((GAS unsigned long long*)p.ws, E, __ATOMIC_RELAXED, __HIP_MEMORY_SCOPE_AGENT);
    __builtin_amdgcn_s_setprio(0);
    FENCE();
  }
  PACE_WAIT(); K_ISSUE(4); PACE_WAIT(); K_ISSUE(5);
  PACE_WAIT(); K_ISSUE(6); PACE_WAIT(); K_ISSUE(7);
  if (w == 0) {
    __builtin_amdgcn_s_setprio(2);
    v4u qp[8];
    for (unsigned spin = 0; spin < SPIN_MAX; ++spin) {
      unsigned m = 0;
#pragma unroll
      for (int i = 0; i < 8; ++i) { qp[i] = LD_SC1(WS_QP + ((X * 4 + (J >> 3)) * 4 + (i >> 1)) * 2048 + ((i & 1) * 64 + lane) * 16); m = max(m, umax4(qp[i])); }
      if (all_lanes(m != SENT)) break;
      asm volatile("" ::: "memory");
    }
#pragma unroll
    for (int hf = 0; hf < 2; ++hf)
#pragma unroll
      for (int ct = 0; ct < 4; ++ct) {
        const float s = (__uint_as_float(qp[hf][ct]) + __uint_as_float(qp[2 + hf][ct])) + (__uint_as_float(qp[4 + hf][ct]) + __uint_as_float(qp[6 + hf][ct]));
        *(LAS unsigned short*)(L + L_Q + (64 * (4 * hf + g) + 16 * ct + n) * 2) = (unsigned short)(pk2(s, s) & 0xffffu);
      }
    asm volatile("s_waitcnt lgkmcnt(0)" ::: "memory");
    if (roleGR) {
      const int gg = 4 * X + (J - J_H);
      v4u gv[8];
      for (unsigned spin = 0; spin < SPIN_MAX; ++spin) {
        bool ok = true;
#pragma unroll
        for (int i = 0; i < 8; ++i) { gv[i] = LD_SC1(WS_MP + (((16 + gg) * 4 + (i >> 1)) * 4 + g) * 512 + ((i & 1) * 16 + n) * 16); ok &= (gv[i].y == ta) & (gv[i].w == tb2); }
        if (all_lanes(ok)) break;
        asm volatile("" ::: "memory");
      }
#pragma unroll
      for (int hf = 0; hf < 2; ++hf) {
        const float s0 = (__uint_as_float(gv[hf].x) + __uint_as_float(gv[2 + hf].x)) + (__uint_as_float(gv[4 + hf].x) + __uint_as_float(gv[6 + hf].x));
        const float s1 = (__uint_as_float(gv[hf].z) + __uint_as_float(gv[2 + hf].z)) + (__uint_as_float(gv[4 + hf].z) + __uint_as_float(gv[6 + hf].z));
        __builtin_amdgcn_raw_buffer_store_b32(nosentu(pk2(s0, s1)), rws, WS_GATE + g * 4096 + gg * 128 + (16 * hf + n) * 4, 0, AUX_SC1);
      }
    }
    __builtin_amdgcn_s_setprio(0);
    FENCE();
  }
  v4u vf[4][8];
#define V_ISSUE(u) do { unsigned vo_ = lane * 16; asm volatile("" : "+v"(vo_)); _Pragma("unroll") for (int j_ = 0; j_ < 8; ++j_) vf[u][j_] = __builtin_amdgcn_raw_buffer_load_b128(rv, vo_ + (j_ & 3) * 1024, kvbase + (2 * (u) + (j_ >> 2)) * 32768, AUX_NT); FENCE(); } while (0)
  PACE_WAIT(); V_ISSUE(0);
  PACE_WAITN(4); V_ISSUE(1);
  bar();
  const v4u qf0 = lds128(L + L_Q + (n & 3) * 256 + g * 32 + ((n >> 2) & 1) * 16), qf1 = lds128(L + L_Q + (n & 3) * 256 + 128 + g * 32 + ((n >> 2) & 1) * 16);
  float S[8][4];
#define QK(t) do { const f32x4 z_ = {0.f, 0.f, 0.f, 0.f}; \
    const f32x4 d0_ = mfma(kf[t][1], qf1, mfma(kf[t][0], qf0, z_)), d1_ = mfma(kf[t][3], qf1, mfma(kf[t][2], qf0, z_)); \
    _Pragma("unroll") for (int r_ = 0; r_ < 4; ++r_) { \
      auto s_ = __builtin_amdgcn_permlane32_swap(__float_as_uint(d0_[r_]), __float_as_uint(d1_[r_]), false, false); \
      S[t][r_] = __uint_as_float(s_[0]) + dpp<ROR4>(__uint_as_float(s_[1])); } } while (0)
  QK(0); QK(1); FENCE(); V_ISSUE(2);
  QK(2); QK(3); FENCE(); V_ISSUE(3);
  if (lane == 0) (void)__hip_atomic_fetch_add((LAS unsigned*)(L + M_CN2), 1u, __ATOMIC_RELAXED, __HIP_MEMORY_SCOPE_WORKGROUP);
  QK(4); QK(5); QK(6); QK(7); FENCE();
  unsigned S2[8][2];
  float mx = -__builtin_inff();
#pragma unroll
  for (int t = 0; t < 8; ++t)
#pragma unroll
    for (int r = 0; r < 2; ++r) {
      const unsigned a = pk2(S[t][2 * r], S[t][2 * r + 1]);
      S2[t][r] = pk2(bf_lo(a) * 0.08838834764831845f, bf_hi(a) * 0.08838834764831845f);
      mx = fmaxf(mx, fmaxf(bf_lo(S2[t][r]), bf_hi(S2[t][r])));
    }
  FENCE();
  mx = x32_max(x16_max(mx));
  float ls = 0.f;
  v4u pf[4];
#pragma unroll
  for (int u = 0; u < 4; ++u) {
    float e[8];
#pragma unroll
    for (int j = 0; j < 8; ++j) { const unsigned sp = S2[2 * u + (j >> 2)][(j & 3) >> 1]; e[j] = __builtin_amdgcn_exp2f((((j & 1) ? bf_hi(sp) : bf_lo(sp)) - mx) * LOG2E); ls += e[j]; }
    pf[u] = v4u{pk2(e[0], e[1]), pk2(e[2], e[3]), pk2(e[4], e[5]), pk2(e[6], e[7])};
  }
  ls = x32_sum(x16_sum(ls));
  if (lane < 4) { *(LAS float*)(L + M_MX + lane * 32 + w * 4) = mx; *(LAS float*)(L + M_LS + lane * 32 + w * 4) = ls; }
  {
    asm volatile("s_waitcnt lgkmcnt(0)" ::: "memory");
    unsigned old = 0;
    if (lane == 0) old = __hip_atomic_fetch_add((LAS unsigned*)(L + M_CNT), 1u, __ATOMIC_RELAXED, __HIP_MEMORY_SCOPE_WORKGROUP);
    if (__builtin_amdgcn_readfirstlane(old) == 7u) {
      const float mw = *(volatile LAS float*)(L + M_MX + ((lane >> 3) & 3) * 32 + (lane & 7) * 4), lw = *(volatile LAS float*)(L + M_LS + ((lane >> 3) & 3) * 32 + (lane & 7) * 4);
      float M = fmaxf(mw, dpp<XOR1>(mw)); M = fmaxf(M, dpp<XOR2>(M)); M = fmaxf(M, dpp<HMIR>(M));
      const float f = __builtin_amdgcn_exp2f((mw - M) * LOG2E);
      float Lc = f * lw; Lc += dpp<XOR1>(Lc); Lc += dpp<XOR2>(Lc); Lc += dpp<HMIR>(Lc);
      if (lane < 32) *(LAS float*)(L + M_FW + lane * 4) = f;
      if (lane < 32 && (lane & 7) == 0) __builtin_amdgcn_raw_buffer_store_b64((v2u{nosent(M), nosent(Lc)}), rws, WS_STAT + cu * 32 + (lane >> 3) * 8, 0, AUX_SC1);
    }
  }
  FENCE();
  v4u wr[8];
#define W_LOADS() do { \
    const int pc_ = 7 * X + (J - J_C); \
    const auto rwa = RSRC(roleB ? p.wo : p.wg, roleB ? 4096 * 512 * 2 : roleC ? 512 * 1792 * 2 : 0); \
    const auto rwb = RSRC(roleB ? p.wo : p.wu, roleB ? 4096 * 512 * 2 : roleC ? 512 * 1792 * 2 : 0); \
    const int rB = 64 * w + (lane >> 3); \
    const int rS = 128 * (w >> 1) + 32 * ((lane >> 5) & 1) + 8 * ((lane >> 3) & 3) + 4 * (w & 1); \
    const unsigned vo = roleB ? ((512 * X + rS) * 512 + 64 * (J - J_B) + 8 * img_chunk(lane, rB)) * 2 : ((64 * w + (lane >> 2)) * 1792 + 32 * pc_ + 8 * (lane & 3)) * 2; \
_Pragma("unroll") \
    for (int e = 0; e < 4; ++e) wr[e] = __builtin_amdgcn_raw_buffer_load_b128(rwa, vo, roleB ? (64 * (e & 1) + (e >> 1)) * 1024 : e * 16 * 1792 * 2, AUX_NT); \
_Pragma("unroll") \
    for (int e = 0; e < 4; ++e) wr[4 + e] = __builtin_amdgcn_raw_buffer_load_b128(rwb, vo, roleB ? (64 * (e & 1) + 2 + (e >> 1)) * 1024 : e * 16 * 1792 * 2, AUX_NT); \
  } while (0)
  f32x4 oa[8];
#pragma unroll
  for (int d = 0; d < 8; ++d) oa[d] = f32x4{0.f, 0.f, 0.f, 0.f};
#define PV(u) do { _Pragma("unroll") for (int d = 0; d < 8; ++d) { \
      const unsigned sel = (d & 1) ? 0x07060302u : 0x05040100u; v4u a; \
      _Pragma("unroll") for (int i = 0; i < 4; ++i) a[i] = __builtin_amdgcn_perm(vf[u][2 * i + 1][d >> 1], vf[u][2 * i][d >> 1], sel); \
      oa[d] = mfma(a, pf[u], oa[d]); } FENCE(); } while (0)
  PV(0); PV(1); PV(2); PV(3);
  if (roleT) { for (unsigned spin = 0; spin < SPIN_LDS; ++spin) { if (*(volatile LAS unsigned*)(L + M_CN2) >= 8u) break; __builtin_amdgcn_s_sleep(1); } }
  W_LOADS(); FENCE();
  int tid2 = threadIdx.x; asm volatile("" : "+v"(tid2));
#define tid tid2
  const int lane2 = tid2 & 63;
#define lane lane2
  const int g2 = lane2 >> 4, n2 = lane2 & 15;
#define g g2
#define n n2
  const int tb = tid2 >> 7, tk = 4 * (tid2 & 127);
  if (n < 4) {
#pragma unroll
    for (int d = 0; d < 8; ++d) *(LAS f32x4*)(L + L_MRG + w * MRG_W + n * MRG_H + d * 64 + g * 16) = oa[d];
  }
  bar();
  {
    const f32x4 f0 = *(LAS f32x4*)(L + M_FW + tb * 32), f1 = *(LAS f32x4*)(L + M_FW + tb * 32 + 16);
    float o = 0.f;
#pragma unroll
    for (int ww = 0; ww < 8; ++ww) o += (ww < 4 ? f0[ww & 3] : f1[ww & 3]) * *(LAS float*)(L + L_MRG + ww * MRG_W + tb * MRG_H + (tid & 127) * 4);
    const float on = dpp<XOR1>(o);
    if ((tid & 1) == 0) __builtin_amdgcn_raw_buffer_store_b32(nosentu(pk2(o, on)), rws, WS_PART + cu * 1024 + tid * 2, 0, AUX_SC1);
  }
  if (!(roleB || roleC)) return;
  v4u wdr[4], gt[2];
  v2u xr;
  if (roleC) {
#pragma unroll
    for (int e = 0; e < 2; ++e) gt[e] = LD_SC1(WS_GATE + ((2 * w + e) * 64 + lane) * 16);
    xr = *(const GAS v2u*)((const GAS unsigned char*)p.x + tid * 8);
    const auto rd = RSRC(p.wd, 1792 * 512 * 2);
    const unsigned o = ((32 * (7 * X + (J - J_C)) + (lane >> 3)) * 512 + 64 * w + 8 * img_chunk(lane, lane >> 3)) * 2;
#pragma unroll
    for (int e = 0; e < 4; ++e) wdr[e] = __builtin_amdgcn_raw_buffer_load_b128(rd, o, e * 8 * 512 * 2, AUX_NT);
  }
  if (roleB) {
#pragma unroll
    for (int e = 0; e < 8; ++e) *(LAS v4u*)(L + L_W + w * 8192 + e * 1024 + lane * 16) = wr[e];
  } else {
    const int rw = lane >> 2, x = (rw >> 1) & 3, c = lane & 3;
    lptr pg = L + L_W + w * 8192 + rw * 128 + ((((c >> 1) ^ x) << 1) | (c & 1)) * 16, pu = L + L_W + w * 8192 + rw * 128 + ((((2 + (c >> 1)) ^ x) << 1) | (c & 1)) * 16;
#pragma unroll
    for (int e = 0; e < 4; ++e) { *(LAS v4u*)(pg + e * 2048) = wr[e]; *(LAS v4u*)(pu + e * 2048) = wr[4 + e]; }
  }
  if (roleB) {
    const int bb = w >> 1, hf = lane >> 5;
    v2u st[2];
    for (unsigned spin = 0; spin < SPIN_MAX; ++spin) {
#pragma unroll
      for (int hh = 0; hh < 2; ++hh) st[hh] = __builtin_amdgcn_raw_buffer_load_b64(rws, WS_STAT + ((8 * bb + 2 * (lane & 3) + hf) * 8 + X) * 32 + (2 * (w & 1) + hh) * 8, 0, AUX_SC1);
      if (all_lanes(max(max(st[0].x, st[0].y), max(st[1].x, st[1].y)) != SENT)) break;
      asm volatile("" ::: "memory");
    }
    float wt[2][4];
#pragma unroll
    for (int hh = 0; hh < 2; ++hh) {
      const float ms = __uint_as_float(st[hh].x);
      float M = fmaxf(ms, dpp<XOR1>(ms)); M = fmaxf(M, dpp<XOR2>(M)); M = x32_max(M);
      const float f = __builtin_amdgcn_exp2f((ms - M) * LOG2E);
      float Ls = f * __uint_as_float(st[hh].y); Ls += dpp<XOR1>(Ls); Ls += dpp<XOR2>(Ls); Ls = x32_sum(Ls);
      const float x = f * fast_rcp(Ls);
      wt[hh][0] = dpp<0x00>(x); wt[hh][1] = dpp<0x55>(x); wt[hh][2] = dpp<0xAA>(x); wt[hh][3] = dpp<0xFF>(x);
    }
    asm volatile("s_waitcnt lgkmcnt(0)" ::: "memory");
    v4u bo[2][4];
#pragma unroll
    for (int ct = 0; ct < 4; ++ct) { lptr wp = L + L_W + w * 8192 + frag_base(lane, ct); bo[0][ct] = w_frag(wp, 0); bo[1][ct] = w_frag(wp, 1); }
    int o_act = L_ACT + bb * RS + act_off(256 * (w & 1) + 4 * (lane & 31));
    int o_a = L_ACT + (lane & 3) * RS + g * 16 + (2 * w) * 64;
    int o_red = L_RED + w * 1024 + g * 256 + n * 4, o_rd = L_RED + lane * 16;
    int o_src = WS_PART + ((8 * bb + hf) * 8 + X) * 1024 + (w & 1) * 512 + (lane & 31) * 8;
    int o_dst = WS_ATT + w * 32768 + ((X * 4 + g) * 512 + 64 * (J - J_B) + 4 * n) * 2;
    PIN(o_act); PIN(o_a); PIN(o_red); PIN(o_rd); PIN(o_src); PIN(o_dst);
    float o[2][4];
    {
      v2u po[8];
      GATHER8(8, po, o_src + (i & 3) * 2 * 8 * 1024 + (i >> 2) * 256);
#pragma unroll
      for (int hh = 0; hh < 2; ++hh) {
        o[hh][0] = x32_sum(wt[hh][0] * bf_lo(po[4 * hh].x) + wt[hh][1] * bf_lo(po[4 * hh + 1].x) + wt[hh][2] * bf_lo(po[4 * hh + 2].x) + wt[hh][3] * bf_lo(po[4 * hh + 3].x));
        o[hh][1] = x32_sum(wt[hh][0] * bf_hi(po[4 * hh].x) + wt[hh][1] * bf_hi(po[4 * hh + 1].x) + wt[hh][2] * bf_hi(po[4 * hh + 2].x) + wt[hh][3] * bf_hi(po[4 * hh + 3].x));
        o[hh][2] = x32_sum(wt[hh][0] * bf_lo(po[4 * hh].y) + wt[hh][1] * bf_lo(po[4 * hh + 1].y) + wt[hh][2] * bf_lo(po[4 * hh + 2].y) + wt[hh][3] * bf_lo(po[4 * hh + 3].y));
        o[hh][3] = x32_sum(wt[hh][0] * bf_hi(po[4 * hh].y) + wt[hh][1] * bf_hi(po[4 * hh + 1].y) + wt[hh][2] * bf_hi(po[4 * hh + 2].y) + wt[hh][3] * bf_hi(po[4 * hh + 3].y));
      }
    }
    if (lane < 32) {
      *(LAS v2u*)(L + o_act) = v2u{pk2(o[0][0], o[0][1]), pk2(o[0][2], o[0][3])};
      *(LAS v2u*)(L + o_act + 256) = v2u{pk2(o[1][0], o[1][1]), pk2(o[1][2], o[1][3])};
    }
    bar();
    {
      const v4u a0 = lds128(L + o_a), a1 = lds128(L + o_a + 64);
#pragma unroll
      for (int ct = 0; ct < 4; ++ct) {
        f32x4 d = mfma(a0, bo[0][ct], f32x4{0.f, 0.f, 0.f, 0.f});
        d = mfma(a1, bo[1][ct], d);
        *(LAS float*)(L + o_red + ct * 64) = sel4(d, g);
      }
    }
    bar();
    {
      f32x4 s0 = *(LAS f32x4*)(L + o_rd), s1 = *(LAS f32x4*)(L + o_rd + 1024);
#pragma unroll
      for (int ww = 2; ww < 8; ww += 2) { s0 += *(LAS f32x4*)(L + o_rd + ww * 1024); s1 += *(LAS f32x4*)(L + o_rd + ww * 1024 + 1024); }
      s0 += s1;
      __builtin_amdgcn_raw_buffer_store_b64((v2u{nosentu(pk2(s0.x, s0.y)), nosentu(pk2(s0.z, s0.w))}), rws, o_dst, 0, AUX_SC1);
    }
    return;
  }
  const int pc = 7 * X + (J - J_C);
#pragma unroll
  for (int e = 0; e < 4; ++e) *(LAS v4u*)(L + L_WD + w * 4096 + e * 1024 + lane * 16) = wdr[e];
  if (!all_lanes(umax4(gt[0]) != SENT && umax4(gt[1]) != SENT)) {
    for (unsigned spin = 0; spin < SPIN_MAX; ++spin) {
      asm volatile("" ::: "memory");
#pragma unroll
      for (int e = 0; e < 2; ++e) gt[e] = LD_SC1(WS_GATE + ((2 * w + e) * 64 + lane) * 16);
      if (all_lanes(umax4(gt[0]) != SENT && umax4(gt[1]) != SENT)) break;
    }
  }
#pragma unroll
  for (int e = 0; e < 2; ++e) *(LAS v4u*)(L + L_GATE + ((2 * w + e) * 64 + lane) * 16) = gt[e];
  if (tid < 4) *(LAS v4u*)(L + M_ZERO + tid * 16) = v4u{0u, 0u, 0u, 0u};
  bar();
  {
    const v2u ga = *(LAS v2u*)(L + L_GATE + tb * 4096 + tk * 2), sb = *(LAS v2u*)(L + L_GATE + tb * 4096 + 1024 + tk * 2), cb = *(LAS v2u*)(L + L_GATE + tb * 4096 + 2048 + tk * 2);
    const v2u xw = xr;
    const float gav[4] = {bf_lo(ga.x), bf_hi(ga.x), bf_lo(ga.y), bf_hi(ga.y)}, sbv[4] = {bf_lo(sb.x), bf_hi(sb.x), bf_lo(sb.y), bf_hi(sb.y)},
                c1v[4] = {rb(1.f + bf_lo(cb.x)), rb(1.f + bf_hi(cb.x)), rb(1.f + bf_lo(cb.y)), rb(1.f + bf_hi(cb.y))}, xs[4] = {bf_lo(xw.x), bf_hi(xw.x), bf_lo(xw.y), bf_hi(xw.y)};
    v4u bg[2][4], bd[4];
#pragma unroll
    for (int ct = 0; ct < 4; ++ct) { lptr wp = L + L_W + w * 8192 + frag_base(lane, ct); bg[0][ct] = w_frag(wp, 0); bg[1][ct] = w_frag(wp, 1); }
    {
      const int q = (lane >> 2) & 3;
#pragma unroll
      for (int kp = 0; kp < 4; ++kp) {
        const int ra = 8 * kp + q, rb_ = ra + 4;
        bd[kp] = tr_frag(L + L_WD + w * 4096 + ra * 128 + ((g ^ ((ra >> 1) & 3)) * 32) + (lane & 3) * 8, L + L_WD + w * 4096 + rb_ * 128 + ((g ^ ((rb_ >> 1) & 3)) * 32) + (lane & 3) * 8);
      }
    }
    int o_src = WS_ATT + X * 32768 + (tb * 512 + tk) * 2;
    int o_x = L_X + tid * 8, o_ln = M_LN + tb * 16, o_act = L_ACT + tb * RS + act_off(tk), o_a = L_ACT + (lane & 3) * RS + g * 16 + (2 * w) * 64;
    int o_red = L_RED + w * 1024 + g * 256 + n * 4, o_rd = L_RED + (2 * w + (lane >> 5)) * 256 + (lane & 31) * 4;
    int o_hw = M_HID + (2 * w + (lane >> 5)) * 64 + (lane & 31) * 2, o_hr = (n >> 2) == g ? M_HID + (n & 3) * 64 : M_ZERO;
    int o_dst = WS_DOWN + pc * 8192 + tid * 16;
    PIN(o_src); PIN(o_x); PIN(o_ln); PIN(o_act); PIN(o_a); PIN(o_red); PIN(o_rd); PIN(o_hw); PIN(o_hr); PIN(o_dst);
    float gv[4] = {gav[0], gav[1], gav[2], gav[3]}, sv[4] = {sbv[0], sbv[1], sbv[2], sbv[3]}, cv1[4] = {c1v[0], c1v[1], c1v[2], c1v[3]}, xx[4] = {xs[0], xs[1], xs[2], xs[3]};
#pragma unroll
    for (int e = 0; e < 4; ++e) { PIN(gv[e]); PIN(sv[e]); PIN(cv1[e]); PIN(xx[e]); }
    float at[4];
    {
      v2u av[8];
      GATHER8(8, av, o_src + i * 4 * 1024);
      at[0] = ((bf_lo(av[0].x) + bf_lo(av[1].x)) + (bf_lo(av[2].x) + bf_lo(av[3].x))) + ((bf_lo(av[4].x) + bf_lo(av[5].x)) + (bf_lo(av[6].x) + bf_lo(av[7].x)));
      at[1] = ((bf_hi(av[0].x) + bf_hi(av[1].x)) + (bf_hi(av[2].x) + bf_hi(av[3].x))) + ((bf_hi(av[4].x) + bf_hi(av[5].x)) + (bf_hi(av[6].x) + bf_hi(av[7].x)));
      at[2] = ((bf_lo(av[0].y) + bf_lo(av[1].y)) + (bf_lo(av[2].y) + bf_lo(av[3].y))) + ((bf_lo(av[4].y) + bf_lo(av[5].y)) + (bf_lo(av[6].y) + bf_lo(av[7].y)));
      at[3] = ((bf_hi(av[0].y) + bf_hi(av[1].y)) + (bf_hi(av[2].y) + bf_hi(av[3].y))) + ((bf_hi(av[4].y) + bf_hi(av[5].y)) + (bf_hi(av[6].y) + bf_hi(av[7].y)));
    }
    unsigned xp[2];
    float x2[4];
#pragma unroll
    for (int e = 0; e < 4; e += 2) {
      const unsigned a = pk2(at[e], at[e + 1]);
      const unsigned t = pk2(gv[e] * bf_lo(a), gv[e + 1] * bf_hi(a));
      xp[e >> 1] = pk2(xx[e] + bf_lo(t), xx[e + 1] + bf_hi(t));
      x2[e] = bf_lo(xp[e >> 1]); x2[e + 1] = bf_hi(xp[e >> 1]);
    }
    float s1 = (x2[0] + x2[1]) + (x2[2] + x2[3]), s2 = (x2[0] * x2[0] + x2[1] * x2[1]) + (x2[2] * x2[2] + x2[3] * x2[3]);
    wave_sum2(s1, s2);
    if (lane == 0) *(LAS v2u*)(L + M_LN + w * 8) = v2u{__float_as_uint(s1), __float_as_uint(s2)};
    *(LAS v2u*)(L + o_x) = v2u{xp[0], xp[1]};
    bar();
    {
      const f32x4 s = *(LAS f32x4*)(L + o_ln);
      float mean_r, rden;
      ln_consts(s.x + s.z, s.y + s.w, mean_r, rden);
      unsigned hp[2];
#pragma unroll
      for (int e = 0; e < 4; e += 2) {
        const unsigned a = pk2(x2[e] - mean_r, x2[e + 1] - mean_r);
        const unsigned t = pk2(bf_lo(a) * rden, bf_hi(a) * rden);
        const unsigned u = pk2(bf_lo(t) * cv1[e], bf_hi(t) * cv1[e + 1]);
        hp[e >> 1] = pk2(bf_lo(u) + sv[e], bf_hi(u) + sv[e + 1]);
      }
      *(LAS v2u*)(L + o_act) = v2u{hp[0], hp[1]};
    }
    bar();
    {
      const v4u a0 = lds128(L + o_a), a1 = lds128(L + o_a + 64);
#pragma unroll
      for (int ct = 0; ct < 4; ++ct) {
        f32x4 d = mfma(a0, bg[0][ct], f32x4{0.f, 0.f, 0.f, 0.f});
        d = mfma(a1, bg[1][ct], d);
        *(LAS float*)(L + o_red + ct * 64) = sel4(d, g);
      }
    }
    bar();
    if (w < 2) {
      float p0[8], p1[8];
#pragma unroll
      for (int ww = 0; ww < 8; ++ww) { p0[ww] = *(LAS float*)(L + o_rd + ww * 1024); p1[ww] = *(LAS float*)(L + o_rd + ww * 1024 + 128); }
      const unsigned gu = pk2(((p0[0] + p0[1]) + (p0[2] + p0[3])) + ((p0[4] + p0[5]) + (p0[6] + p0[7])), ((p1[0] + p1[1]) + (p1[2] + p1[3])) + ((p1[4] + p1[5]) + (p1[6] + p1[7])));
      const float sg = bf_lo(gu), up = bf_hi(gu);
      const float u = sg * (1.f + 0.044715f * sg * sg) * (-2.f * LOG2E * 0.7978845608028654f);
      const float ge = rb(sg * fast_rcp(1.f + __builtin_amdgcn_exp2f(u)));
      *(LAS unsigned short*)(L + o_hw) = (unsigned short)(pk2(ge * up, 0.f) & 0xffffu);
    }
    bar();
    {
      const v4u h0 = lds128(L + o_hr), h1 = lds128(L + o_hr + 16), h2 = lds128(L + o_hr + 32), h3 = lds128(L + o_hr + 48);
      const f32x4 z = {0.f, 0.f, 0.f, 0.f};
      const f32x4 d = mfma(h1, bd[1], mfma(h0, bd[0], z)) + mfma(h3, bd[3], mfma(h2, bd[2], z));
      ST_SC1((v4u{nosent(d[0]), nosent(d[1]), nosent(d[2]), nosent(d[3])}), o_dst);
    }
  }
  if (J < J_F_END) {
    const int f = 4 * X + (J - J_C);
    float gb = bf_lo(*(LAS unsigned short*)(L + L_GATE + g * 4096 + 3072 + (16 * f + n) * 2));
    float x2 = bf_lo(*(LAS unsigned short*)(L + L_X + (g * 512 + 16 * f + n) * 2));
    int o_src = WS_DOWN + (4 * w + g) * 8192 + (16 * f + n) * 16, o_wr = L_RED + w * 256 + lane * 16, o_rd = L_RED + n * 16 + g * 4, o_out = (g * 512 + 16 * f + n) * 2;
    PIN(gb); PIN(x2); PIN(o_src); PIN(o_wr); PIN(o_rd); PIN(o_out);
    if (w < 7) {
      f32x4 s;
      v4u dv[2];
      GATHER(2, dv, o_src + i * 28 * 8192);
#pragma unroll
      for (int b = 0; b < 4; ++b) s[b] = x32_sum(x16_sum(__uint_as_float(dv[0][b]) + __uint_as_float(dv[1][b])));
      if (lane < 16) *(LAS f32x4*)(L + o_wr) = s;
    }
    bar();
    if (w == 0) {
      float q7[7];
#pragma unroll
      for (int ww = 0; ww < 7; ++ww) q7[ww] = *(LAS float*)(L + o_rd + ww * 256);
      const float y = rb(((q7[0] + q7[1]) + (q7[2] + q7[3])) + ((q7[4] + q7[5]) + q7[6]));
      const unsigned ob = pk2(x2 + rb(gb * y), 0.f) & 0xffffu;
      const unsigned o2 = ob | (dppu<XOR1>(ob) << 16);
      if ((n & 1) == 0) *(GAS unsigned*)((GAS unsigned char*)p.out + o_out) = o2;
    }
  }
#undef tid
#undef lane
#undef g
#undef n
}
}

extern "C" void kernel_launch(void* const* d_in, const int* in_sizes, int n_in, void* d_out, int out_size, void* d_ws, size_t ws_size, hipStream_t stream) {
  (void)in_sizes; (void)n_in; (void)out_size; (void)ws_size;
  Params p;
  p.x = d_in[0]; p.k = d_in[1]; p.v = d_in[2]; p.wq = d_in[3]; p.wo = d_in[4];
  p.wg = d_in[6]; p.wu = d_in[7]; p.wd = d_in[8]; p.c = d_in[9]; p.wm = d_in[10];
  p.out = d_out; p.ws = d_ws;
  hipLaunchKernelGGL(adaln_block_kernel, dim3(NCU), dim3(NTHR), 0, stream, p);
}
```
